```python
import torch

def _softmax(x, axis=-1):
    return torch.softmax(x, dim=axis)
def _gelu(x):
    return torch.nn.functional.gelu(x, approximate="tanh")
def _silu(x):
    return torch.nn.functional.silu(x)
def _sigmoid(x):
    return torch.sigmoid(x)
def _layernorm(x, axis=-1, eps=1e-5):
    m = x.mean(dim=axis, keepdim=True)
    v = x.var(dim=axis, keepdim=True, unbiased=False)
    return (x - m) / torch.sqrt(v + eps)
def _scatter_add(tgt, idx, src, axis=0):
    return tgt.clone().index_add_(axis, idx, src)
def _segment_sum(src, seg, n):
    out = torch.zeros((n,) + src.shape[1:], dtype=src.dtype)
    return out.index_add_(0, seg, src)
def _pad(x, pw):
    flat = tuple(v for pair in reversed(pw) for v in pair)
    return torch.nn.functional.pad(x, flat)

kernel_name = 'sequential_preLN__sliding_block__moe_arch_mod__prompt'

def setup_inputs():

    _w = (lambda a: (lambda t: t.to(torch.bfloat16) if t.is_floating_point() else t)(torch.as_tensor(a)))
    def _A_setup():
        B, S, D = 1, 2048, 512
        return {
            "x": _w((lambda *s: torch.randn(*s, dtype=torch.bfloat16) * 0.05)(B, S, D)),
            "wqkv": _w((lambda *s: torch.randn(*s, dtype=torch.bfloat16) * 0.05)(D, 3 * D)),
            "wo": _w((lambda *s: torch.randn(*s, dtype=torch.bfloat16) * 0.05)(D, D)),
        }
    _A_in = _A_setup()

    def _B_setup():
        B, S, D, F, C = 1, 2048, 512, 1376, 2
        return {
            "x": _w((lambda *s: torch.randn(*s, dtype=torch.bfloat16) * 0.05)(B, S, D)),
            "w_r": _w((lambda *s: torch.randn(*s, dtype=torch.bfloat16) * 0.05)(D)),
            "w_gate": _w((lambda *s: torch.randn(*s, dtype=torch.bfloat16) * 0.05)(D, F)),
            "w_up": _w((lambda *s: torch.randn(*s, dtype=torch.bfloat16) * 0.05)(D, F)),
            "w_down": _w((lambda *s: torch.randn(*s, dtype=torch.bfloat16) * 0.05)(F, D)),
        }
    _B_in = _B_setup()

    return {
        "A_x": _A_in["x"],
        "A_wqkv": _A_in["wqkv"],
        "A_wo": _A_in["wo"],
        "B_x": _B_in["x"],
        "B_w_r": _B_in["w_r"],
        "B_w_gate": _B_in["w_gate"],
        "B_w_up": _B_in["w_up"],
        "B_w_down": _B_in["w_down"],
    }

def reference(A_x, A_wqkv, A_wo, B_x, B_w_r, B_w_gate, B_w_up, B_w_down):
    def _A_ref(x, wqkv, wo):
        B, S, D = x.shape
        H = 32; Dh = D // H; W = 256
        qkv = (lambda x, *d: x.permute(*d))((x @ wqkv).reshape(B, S, 3, H, Dh), 2, 0, 3, 1, 4)
        q, k, v = qkv[0], qkv[1], qkv[2]
        scores = torch.einsum("bhqd,bhkd->bhqk", q, k) * (Dh ** -0.5)
        idx = torch.arange(S)
        mask = (idx[:, None] - idx[None, :] >= 0) & (idx[:, None] - idx[None, :] < W)
        scores = torch.where(mask[None, None], scores, -1e9)
        p = _softmax(scores, axis=-1)
        o = torch.einsum("bhqk,bhkd->bhqd", p, v)
        return (lambda x, *d: x.permute(*d))(o, 0, 2, 1, 3).reshape(B, S, D) @ wo

    def _B_ref(x, w_r, w_gate, w_up, w_down):
        B, S, D = x.shape
        C = 2
        r = x @ w_r
        idx = torch.argsort(-r, axis=-1)[:, :C]
        idx_e = (lambda x, n, axis: torch.repeat_interleave(x, n, dim=axis))(idx.reshape(B, C, 1), D, axis=2)
        x_sel = (lambda x, i, axis: torch.take_along_dim(x, i, dim=axis))(x, idx_e, axis=1)
        g = _silu(x_sel @ w_gate)
        u = x_sel @ w_up
        y = (g * u) @ w_down
        r_sel = _sigmoid((lambda x, i, axis: torch.take_along_dim(x, i, dim=axis))(r, idx, axis=1))
        y = y * r_sel.reshape(B, C, 1)
        b_off = (torch.arange(B) * S).reshape(B, 1)
        flat_idx = (idx + b_off).reshape(B * C)
        zero = (x * 0.0).reshape(B * S, D)
        upd = _scatter_add(zero, flat_idx, y.reshape(B * C, D), axis=0)
        return x + upd.reshape(B, S, D)

    x = A_x
    h = x + _A_ref(_layernorm(x, axis=-1), A_wqkv, A_wo)
    h2 = h + _B_ref(_layernorm(h, axis=-1), B_w_r, B_w_gate, B_w_up, B_w_down)
    return h2
```

```cpp
#include <hip/hip_runtime.h>
#include <hip/hip_bf16.h>
#include <stdint.h>

typedef unsigned short u16;
__device__ __forceinline__ float bf2f(u16 b) { return __uint_as_float(((unsigned)b) << 16); }
__device__ __forceinline__ u16 f2bf(float f) { __hip_bfloat16 h = __float2bfloat16(f); return *(u16*)&h; }

constexpr int S = 2048, D = 512, H = 32, DH = 16, W = 256, F = 1376;

__global__ void ln_kernel(const u16* __restrict__ x, u16* __restrict__ xn) {
    int row = blockIdx.x; int lane = threadIdx.x;
    float v[8]; float s = 0;
    for (int j = 0; j < 8; ++j) { v[j] = bf2f(x[row * D + lane * 8 + j]); s += v[j]; }
    for (int m = 32; m >= 1; m >>= 1) s += __shfl_xor(s, m);
    float mean = s / D; float q = 0;
    for (int j = 0; j < 8; ++j) { float d = v[j] - mean; q += d * d; }
    for (int m = 32; m >= 1; m >>= 1) q += __shfl_xor(q, m);
    float rstd = rsqrtf(q / D + 1e-5f);
    for (int j = 0; j < 8; ++j) xn[row * D + lane * 8 + j] = f2bf((v[j] - mean) * rstd);
}
__global__ void gemm_kernel(const u16* __restrict__ A, const u16* __restrict__ B, u16* __restrict__ C, int N, int K) {
    int n = blockIdx.x * 64 + threadIdx.x; int m0 = blockIdx.y * 8;
    float acc[8] = {0};
    for (int k = 0; k < K; ++k) { float b = bf2f(B[k * N + n]);
        for (int i = 0; i < 8; ++i) acc[i] += bf2f(A[(m0 + i) * K + k]) * b; }
    for (int i = 0; i < 8; ++i) C[(m0 + i) * N + n] = f2bf(acc[i]);
}
__global__ void attn_kernel(const u16* __restrict__ qkv, u16* __restrict__ o) {
    int q = blockIdx.x * 64 + threadIdx.x; int h = blockIdx.y;
    float qv[16]; for (int d = 0; d < 16; ++d) qv[d] = bf2f(qkv[q * 1536 + h * 16 + d]);
    int k0 = q - 255 < 0 ? 0 : q - 255;
    float m = -1e30f;
    for (int k = k0; k <= q; ++k) { float s = 0; for (int d = 0; d < 16; ++d) s += qv[d] * bf2f(qkv[k * 1536 + 512 + h * 16 + d]); s *= 0.25f; m = fmaxf(m, s); }
    float l = 0; float acc[16] = {0};
    for (int k = k0; k <= q; ++k) { float s = 0; for (int d = 0; d < 16; ++d) s += qv[d] * bf2f(qkv[k * 1536 + 512 + h * 16 + d]); s *= 0.25f; float p = __expf(s - m); l += p;
        for (int d = 0; d < 16; ++d) acc[d] += p * bf2f(qkv[k * 1536 + 1024 + h * 16 + d]); }
    for (int d = 0; d < 16; ++d) o[q * D + h * 16 + d] = f2bf(acc[d] / l);
}
__global__ void ln2_kernel(const u16* __restrict__ x, const u16* __restrict__ ao, const u16* __restrict__ wr, u16* __restrict__ hn, float* __restrict__ r, u16* __restrict__ out) {
    int row = blockIdx.x; int lane = threadIdx.x;
    float v[8]; float s = 0;
    for (int j = 0; j < 8; ++j) { v[j] = bf2f(f2bf(bf2f(x[row * D + lane * 8 + j]) + bf2f(ao[row * D + lane * 8 + j]))); s += v[j]; }
    for (int m = 32; m >= 1; m >>= 1) s += __shfl_xor(s, m);
    float mean = s / D; float q = 0;
    for (int j = 0; j < 8; ++j) { float d = v[j] - mean; q += d * d; }
    for (int m = 32; m >= 1; m >>= 1) q += __shfl_xor(q, m);
    float rstd = rsqrtf(q / D + 1e-5f); float rr = 0;
    for (int j = 0; j < 8; ++j) { u16 b = f2bf((v[j] - mean) * rstd); hn[row * D + lane * 8 + j] = b; rr += bf2f(b) * bf2f(wr[lane * 8 + j]); out[row * D + lane * 8 + j] = f2bf(v[j] + bf2f(b)); }
    for (int m = 32; m >= 1; m >>= 1) rr += __shfl_xor(rr, m);
    if (lane == 0) r[row] = rr;
}
__global__ void ffn_kernel(const u16* __restrict__ x, const u16* __restrict__ ao, const u16* __restrict__ hn, const float* __restrict__ r, const u16* __restrict__ wg, const u16* __restrict__ wu, const u16* __restrict__ wd, u16* __restrict__ out) {
    __shared__ int idx[2]; __shared__ float act[2][F];
    int t = threadIdx.x;
    if (t == 0) { int b0 = -1, b1 = -1; float v0 = -INFINITY, v1 = -INFINITY;
        for (int i = 0; i < S; ++i) { float v = r[i]; if (v > v0 || b0 < 0) { v1 = v0; b1 = b0; v0 = v; b0 = i; } else if (v > v1 || b1 < 0) { v1 = v; b1 = i; } }
        idx[0] = b0; idx[1] = b1; }
    __syncthreads();
    for (int f = t; f < F; f += blockDim.x) for (int c = 0; c < 2; ++c) { float g = 0, u = 0; const u16* xr = hn + idx[c] * D;
        for (int k = 0; k < D; ++k) { float xv = bf2f(xr[k]); g += xv * bf2f(wg[k * F + f]); u += xv * bf2f(wu[k * F + f]); }
        g = bf2f(f2bf(g)); u = bf2f(f2bf(u)); float sg = bf2f(f2bf(g / (1.f + __expf(-g)))); act[c][f] = bf2f(f2bf(sg * u)); }
    __syncthreads();
    for (int n = t; n < D; n += blockDim.x) for (int c = 0; c < 2; ++c) { float y = 0; for (int f = 0; f < F; ++f) y += act[c][f] * bf2f(wd[f * D + n]);
        float rv = bf2f(f2bf(r[idx[c]])); float sg = 1.f / (1.f + __expf(-rv)); y = bf2f(f2bf(y)) * bf2f(f2bf(sg));
        int row = idx[c]; float hh = bf2f(f2bf(bf2f(x[row * D + n]) + bf2f(ao[row * D + n]))); float hnv = bf2f(hn[row * D + n]);
        out[row * D + n] = f2bf(hh + bf2f(f2bf(hnv + bf2f(f2bf(y))))); }
}

extern "C" void kernel_launch(void* const* d_in, const int* in_sizes, int n_in, void* d_out, int out_size, void* d_ws, size_t ws_size, hipStream_t stream) {
    const u16* x = (const u16*)d_in[0]; const u16* wqkv = (const u16*)d_in[1]; const u16* wo = (const u16*)d_in[2];
    const u16* wr = (const u16*)d_in[4]; const u16* wg = (const u16*)d_in[5]; const u16* wu = (const u16*)d_in[6]; const u16* wd = (const u16*)d_in[7];
    u16* out = (u16*)d_out; char* ws = (char*)d_ws;
    u16* xn = (u16*)ws; u16* qkv = (u16*)(ws + (4 << 20)); u16* o = (u16*)(ws + (16 << 20)); u16* ao = (u16*)(ws + (20 << 20)); u16* hn = (u16*)(ws + (24 << 20)); float* r = (float*)(ws + (28 << 20));
    ln_kernel<<<S, 64, 0, stream>>>(x, xn);
    gemm_kernel<<<dim3(1536 / 64, S / 8), 64, 0, stream>>>(xn, wqkv, qkv, 1536, 512);
    attn_kernel<<<dim3(S / 64, H), 64, 0, stream>>>(qkv, o);
    gemm_kernel<<<dim3(512 / 64, S / 8), 64, 0, stream>>>(o, wo, ao, 512, 512);
    ln2_kernel<<<S, 64, 0, stream>>>(x, ao, wr, hn, r, out);
    ffn_kernel<<<1, 512, 0, stream>>>(x, ao, hn, r, wg, wu, wd, out);
}
```

```cpp
#include <hip/hip_runtime.h>
#include <stdint.h>

typedef unsigned short u16;
typedef unsigned u32;
typedef unsigned long long u64;
typedef __bf16 bf16x2 __attribute__((ext_vector_type(2)));
typedef __bf16 bf16x8 __attribute__((ext_vector_type(8)));
typedef short s16x4 __attribute__((ext_vector_type(4)));
typedef float f32x2 __attribute__((ext_vector_type(2)));
typedef float f32x4 __attribute__((ext_vector_type(4)));
typedef float f32x16 __attribute__((ext_vector_type(16)));
typedef u32 u32x2 __attribute__((ext_vector_type(2)));
typedef u32 u32x4 __attribute__((ext_vector_type(4)));
#define LAS __attribute__((address_space(3)))
#define GAS __attribute__((address_space(1)))
#define DI __device__ __forceinline__
#define RLX_AGENT __ATOMIC_RELAXED, __HIP_MEMORY_SCOPE_AGENT
#define SC1 16

#define XMAP 8
#define STAMPS 0

constexpr int FF = 1376;
constexpr int NFS = 43;
constexpr u32 SPIN_CAP = 1u << 15;
constexpr u32 WS_EPOCH = 0;
constexpr u32 WS_FLAGA = 0x1000;
constexpr u32 WS_FLAGB = WS_FLAGA + 256 * 128;
constexpr u32 WS_CAND = WS_FLAGB + 256 * 128;
constexpr u32 WS_ROWS = WS_CAND + 32 * 128;
constexpr u32 WS_STAT = WS_ROWS + 32 * 4096;
constexpr u32 WS_Y = WS_STAT + 256 * 2048;
constexpr u32 WS_KV = 0x200000;
constexpr u32 WS_O = WS_KV + 256 * 16384;
constexpr u32 WS_END = WS_O + 2048 * 1024;
constexpr u32 WS_STAMP = 0x1000000;
static_assert(WS_Y + NFS * 8192 <= WS_KV, "ws map");
constexpr u32 L_X = 0, L_R = 65536, L_M = L_R + 5 * 16384, LDS_BYTES = 163840;
constexpr u32 L_S2 = L_R + 2 * 16384;
constexpr u32 L_TAB = L_M;
constexpr u32 L_CNT = L_M + 1024;
constexpr u32 L_VT = L_M + 2048;
constexpr u32 L_Q = L_M + 2048;

typedef __amdgpu_buffer_rsrc_t rsrc_t;
DI u32 rfl(u32 v) { return __builtin_amdgcn_readfirstlane(v); }
DI rsrc_t mkrs(const void* p, u32 n) { return __builtin_amdgcn_make_buffer_rsrc((void*)p, 0, n, 0x00020000); }
template <u32 IMM> DI void dma1(u32 wb, u32 voff, rsrc_t rs, u32 soff) {
    asm volatile("s_add_u32 m0, %0, %4\n\ts_nop 0\n\tbuffer_load_dwordx4 %1, %2, %3 offen lds" :: "s"(wb), "v"(voff), "s"(rs), "s"(soff), "i"(IMM) : "scc", "memory");
}
template <u32 IMM> DI void dma2(u32 wb, u32 voff, rsrc_t rs, u32 soff) {
    asm volatile("s_add_u32 m0, %0, %4\n\ts_nop 0\n\tbuffer_load_dwordx4 %1, %2, %3 offen lds\n\tbuffer_load_dwordx4 %1, %2, %3 offen offset:1024 lds"
                 :: "s"(wb), "v"(voff), "s"(rs), "s"(soff), "i"(IMM) : "scc", "memory");
}
#define SB() __builtin_amdgcn_sched_barrier(0)
#define PINV(x) asm volatile("" : "+v"(x))
#define PINS(x) asm volatile("" : "+s"(x))
#define BAR() asm volatile("s_waitcnt lgkmcnt(0)\n\ts_barrier" ::: "memory")
template <int N> DI void waitbar() { asm volatile("s_waitcnt vmcnt(%0) lgkmcnt(0)\n\ts_barrier" :: "i"(N) : "memory"); }
#define WAITV(n) asm volatile("s_waitcnt vmcnt(" #n ")" ::: "memory")
#define MFMA32(a, b, c) __builtin_amdgcn_mfma_f32_32x32x16_bf16(__builtin_bit_cast(bf16x8, a), __builtin_bit_cast(bf16x8, b), c, 0, 0, 0)
#define MFMA16(a, b, c) __builtin_amdgcn_mfma_f32_16x16x32_bf16(__builtin_bit_cast(bf16x8, a), __builtin_bit_cast(bf16x8, b), c, 0, 0, 0)
#define LD128(off, soff) __builtin_amdgcn_raw_buffer_load_b128(wsr, off, soff, SC1)
#define LD64(off, soff) __builtin_amdgcn_raw_buffer_load_b64(wsr, off, soff, SC1)
#define LD32(off, soff) __builtin_amdgcn_raw_buffer_load_b32(wsr, off, soff, SC1)
#define ST128(v, off, soff) asm volatile("buffer_store_dwordx4 %0, %1, %2, %3 offen sc1\n\ts_nop 1" :: "v"(v), "v"(off), "s"(wsr), "s"(soff) : "memory")
#define ST64(v, off, soff) __builtin_amdgcn_raw_buffer_store_b64(v, wsr, off, soff, SC1)
DI u32x4 lds128(u32 a) { return *(const LAS u32x4*)(uintptr_t)a; }
DI u32x2 lds64(u32 a) { return *(const LAS u32x2*)(uintptr_t)a; }
DI u32 lds32(u32 a) { return *(const LAS u32*)(uintptr_t)a; }
DI f32x4 ldsf4(u32 a) { return *(const LAS f32x4*)(uintptr_t)a; }
DI void sts128(u32 a, u32x4 v) { *(LAS u32x4*)(uintptr_t)a = v; }
DI void sts64(u32 a, u32x2 v) { *(LAS u32x2*)(uintptr_t)a = v; }
DI void sts32(u32 a, u32 v) { *(LAS u32*)(uintptr_t)a = v; }
DI void stsf4(u32 a, f32x4 v) { *(LAS f32x4*)(uintptr_t)a = v; }
DI void stsf(u32 a, float v) { *(LAS float*)(uintptr_t)a = v; }
DI float ldsf(u32 a) { return *(const LAS float*)(uintptr_t)a; }
DI u32x2 ldstr(u32 a) { return __builtin_bit_cast(u32x2, __builtin_amdgcn_ds_read_tr16_b64_v4i16((LAS s16x4*)(uintptr_t)a)); }
DI u32x4 trfrag(u32 a0, u32 a1) { u32x2 lo = ldstr(a0), hi = ldstr(a1); return (u32x4){lo.x, lo.y, hi.x, hi.y}; }
DI u32 pk(float a, float b) { f32x2 v = {a, b}; return __builtin_bit_cast(u32, __builtin_convertvector(v, bf16x2)); }
DI float dot2(u32 a, u32 b, float c) { return __builtin_amdgcn_fdot2_f32_bf16(__builtin_bit_cast(bf16x2, a), __builtin_bit_cast(bf16x2, b), c, false); }
DI float bflo(u32 v) { return __uint_as_float(v << 16); }
DI float bfhi(u32 v) { return __uint_as_float(v & 0xffff0000u); }
DI float rbf(float v) { return bfhi(pk(v, v)); }
DI float xhalf_max(float v) { auto r = __builtin_amdgcn_permlane32_swap(__float_as_uint(v), __float_as_uint(v), false, false); return fmaxf(__uint_as_float(r[0]), __uint_as_float(r[1])); }
constexpr u32 ONE2 = 0x3f803f80u;
DI u32x4 pack8(const f32x16& v, int o) { return (u32x4){pk(v[o], v[o + 1]), pk(v[o + 2], v[o + 3]), pk(v[o + 4], v[o + 5]), pk(v[o + 6], v[o + 7])}; }
DI float max16(const f32x16& s, float m) {
#pragma unroll
    for (int r = 0; r < 16; r += 2) m = __builtin_fmaxf(__builtin_fmaxf(m, s[r]), s[r + 1]);
    return m;
}
template <int CTRL> DI float dppadd(float v) { return v + __int_as_float(__builtin_amdgcn_update_dpp(0, __float_as_int(v), CTRL, 0xf, 0xf, true)); }
DI float sum8(float v) { v = dppadd<0xB1>(v); v = dppadd<0x4E>(v); return dppadd<0x141>(v); }
template <int CTRL> DI u32 dppmax(u32 v) { return max(v, (u32)__builtin_amdgcn_update_dpp(0, (int)v, CTRL, 0xf, 0xf, true)); }
DI u32 wave_umax(u32 v) {
    v = dppmax<0x111>(v); v = dppmax<0x112>(v); v = dppmax<0x114>(v); v = dppmax<0x118>(v); v = dppmax<0x142>(v); v = dppmax<0x143>(v);
    return __builtin_amdgcn_readlane(v, 63);
}
DI u32 fkey(float f) { u32 u = __float_as_uint(f); return u ^ ((u32)((int)u >> 31) | 0x80000000u); }
DI float fkey_inv(u32 k) { u32 u = (k & 0x80000000u) ? (k ^ 0x80000000u) : ~k; return __uint_as_float(u); }

#define STAMP(i) do {} while (0)

__global__ __launch_bounds__(512, 2) void fused_block(const u16* __restrict__ x, const u16* __restrict__ wqkv, const u16* __restrict__ wo,
                                                      const u16* __restrict__ wr, const u16* __restrict__ wg, const u16* __restrict__ wu,
                                                      const u16* __restrict__ wd, u16* __restrict__ out, const u32* __restrict__ epoch_rd, char* ws) {
    STAMP(0);
    const u32 b = blockIdx.x, tid = threadIdx.x, lane = tid & 63, wid = rfl(tid >> 6);
    const u32 xcd = b & 7, rr = b >> 3;
    const u32 tb = xcd * 4 + (rr & 3), g = rr >> 2, rot = 2 * (rr & 3) + g;
    const u32 wgi = tb * 8 + g;
    const u32 l31 = lane & 31, hf = lane >> 5, l7 = lane & 7, l3 = lane >> 3;
    const u32 q4 = (lane & 15) >> 2, p4 = lane & 3, blk = (lane >> 4) & 1;
    const u32 wsw = (l7 ^ (blk << 2)) << 4;
    const u32 trc = (blk * 32 + p4 * 8) ^ ((q4 >> 1) << 6);
    const rsrc_t rw = mkrs((const char*)wqkv + g * 128, 512 * 3072 - g * 128);
    const rsrc_t wsr = mkrs(ws, WS_END);
#define XOFF(c) ((((c) + rot) & 7) * 128)
#define MF(D, A, B, C) do { SB(); D = MFMA32(A, B, C); PINV(D); SB(); } while (0)
    f32x16 zt;
#pragma unroll
    for (int i = 0; i < 16; ++i) zt[i] = 0.f;
    u32 tag;
    const u32 tt = wid & 1;
    u32 xa[4];
    u32 wa0, wa1;
#define WADDR(s, o) (((s) % 5) < 3 ? wa0 + ((s) % 5) * 16384 + (o) : wa1 + (((s) % 5) - 3) * 16384 + (o))

    if (wid >= 4) {
        const u32 hw = wid - 4;
        const rsrc_t rx = mkrs((const char*)x + (size_t)tb * 65536, 65536);
        const u32 r0 = hw * 16 + l3;
        const u32 vx0 = r0 * 1024 + ((l7 ^ ((r0 >> 1) & 7)) << 4), vx1 = (r0 + 8) * 1024 + ((l7 ^ (((r0 + 8) >> 1) & 7)) << 4);
        const u32 vw0 = r0 * 3072 + wsw, vw1 = vw0 + 8 * 3072;
        const u32 wb1 = hw * 2048, wb2 = hw * 4096;
#define XD(c) do { dma1<L_X + (c) * 8192>(wb1, vx0, rx, XOFF(c)); dma1<L_X + (c) * 8192 + 1024>(wb1, vx1, rx, XOFF(c)); } while (0)
#define STEP(s) do { \
        if ((s) < 4) XD(2 * (s)); \
        if ((s) < 8) { dma2<L_R + ((s) % 5) * 16384>(wb2, vw0, rw, XOFF(s) * 1536 + 1024); dma2<L_R + ((s) % 5) * 16384 + 2048>(wb2, vw1, rw, XOFF(s) * 1536 + 1024); } \
        else { dma1<L_R + ((s) % 5) * 16384>(wb2, vw0, rw, XOFF(2 * ((s) - 8)) * 1536); dma1<L_R + ((s) % 5) * 16384 + 2048>(wb2, vw1, rw, XOFF(2 * ((s) - 8)) * 1536); \
               dma1<L_R + ((s) % 5) * 16384 + 1024>(wb2, vw0, rw, XOFF(2 * ((s) - 8) + 1) * 1536); dma1<L_R + ((s) % 5) * 16384 + 3072>(wb2, vw1, rw, XOFF(2 * ((s) - 8) + 1) * 1536); } \
        if ((s) < 4) XD(2 * (s) + 1); \
    } while (0)
        STEP(0); STEP(1); STEP(2);
        tag = epoch_rd[0] + 1u;
        u32 sa = L_X + hw * 2048 + lane * 16;
        { const u32 row = tt * 32 + l31;
#pragma unroll
          for (int ks = 0; ks < 4; ++ks) xa[ks] = L_X + row * 128 + (((2 * ks + hf) ^ ((row >> 1) & 7)) << 4); }
        wa0 = L_R + hf * 2048 + q4 * 128 + (trc ^ ((hw >> 1) << 6)); wa1 = wa0 + 3 * 16384;
        PINV(xa[0]); PINV(xa[1]); PINV(xa[2]); PINV(xa[3]); PINV(wa0); PINV(wa1); PINV(sa);
        asm volatile("" :: "s"(tag));
        float sx0 = 0.f, sq0 = 0.f, sx1 = 0.f, sq1 = 0.f;
#define STATS(j) do { const u32x4 a_ = lds128(sa + (2 * (j)) * 8192), b_ = lds128(sa + (2 * (j)) * 8192 + 1024), c_ = lds128(sa + (2 * (j) + 1) * 8192), d_ = lds128(sa + (2 * (j) + 1) * 8192 + 1024); \
        _Pragma("unroll") for (int e = 0; e < 4; ++e) { sx0 = dot2(a_[e], ONE2, sx0); sq0 = dot2(a_[e], a_[e], sq0); sx1 = dot2(b_[e], ONE2, sx1); sq1 = dot2(b_[e], b_[e], sq1); } \
        _Pragma("unroll") for (int e = 0; e < 4; ++e) { sx0 = dot2(c_[e], ONE2, sx0); sq0 = dot2(c_[e], c_[e], sq0); sx1 = dot2(d_[e], ONE2, sx1); sq1 = dot2(d_[e], d_[e], sq1); } \
        PINV(sx0); PINV(sq0); PINV(sx1); PINV(sq1); SB(); } while (0)
        waitbar<16>(); STEP(3); STATS(0);
        waitbar<16>(); STEP(4); STATS(1);
        waitbar<12>(); STEP(5); STATS(2);
        waitbar<8>(); STEP(6); STATS(3);
        waitbar<8>(); STEP(7);
        {
            sx0 = sum8(sx0); sq0 = sum8(sq0); sx1 = sum8(sx1); sq1 = sum8(sq1);
            const float sx = l7 ? sx1 : sx0, sq = l7 ? sq1 : sq0;
            const float nmean = sx * (-1.f / 512.f);
            const float rs = __builtin_amdgcn_rsqf(fmaxf(sq * (1.f / 512.f) - nmean * nmean, 0.f) + 1e-5f);
            const u32 mh = pk(nmean, nmean);
            if (l7 < 2) sts128(L_TAB + (r0 + l7 * 8) * 16, (u32x4){__float_as_uint(rs), pk(nmean, nmean - bfhi(mh)), mh & 0xffffu, 0u});
            if (tid == 256) sts32(L_CNT, 0u);
        }
        waitbar<8>(); STEP(8);
        waitbar<8>(); STEP(9);
        waitbar<8>(); STEP(10);
        const u32x4 tabv = lds128(L_TAB + (tt * 32 + l31) * 16);
        asm volatile("s_barrier" ::: "memory");
        STEP(11);
        waitbar<0>();
        f32x16 acc = zt; float cq = 0.f;
        u32x4 xf[3], wf[3];
#define QRD(n) do { xf[(n) % 3] = lds128(xa[(n) & 3] + ((n) >> 2) * 8192); \
            wf[(n) % 3] = trfrag(WADDR(8 + (n) / 8, (((n) >> 2) & 1) * 1024 + ((n) & 3) * 4096), WADDR(8 + (n) / 8, (((n) >> 2) & 1) * 1024 + ((n) & 3) * 4096 + 512)); SB(); } while (0)
#define QCP(n) do { MF(acc, wf[(n) % 3], xf[(n) % 3], acc); _Pragma("unroll") for (int e = 0; e < 4; ++e) cq = dot2(wf[(n) % 3][e], ONE2, cq); PINV(cq); SB(); } while (0)
        QRD(0); QRD(1);
#pragma unroll
        for (int n = 0; n < 32; ++n) { if (n + 2 < 32) QRD(n + 2); QCP(n); }
        const u32 ch = pk(cq, cq);
        acc = MFMA32(((u32x4){ch, pk(cq - bfhi(ch), 0.f), 0u, 0u}), ((u32x4){tabv.y, tabv.z, 0u, 0u}), acc);
        const float qs = __uint_as_float(tabv.x) * (0.25f * 1.4426950408889634f);
#pragma unroll
        for (int i = 0; i < 16; ++i) acc[i] *= qs;
        sts128(L_Q + (4 * (hw >> 1) + tt) * 1024 + lane * 16, pack8(acc, 0));
        sts128(L_Q + (4 * (hw >> 1) + 2 + tt) * 1024 + lane * 16, pack8(acc, 8));
    } else {
        tag = epoch_rd[0] + 1u;
#pragma unroll
        for (int ks = 0; ks < 4; ++ks) xa[ks] = L_X + l31 * 128 + (((2 * ks + hf) ^ ((l31 >> 1) & 7)) << 4);
        wa0 = L_R + hf * 2048 + (wid >> 1) * 1024 + q4 * 128 + (trc ^ ((wid & 1) << 6)); wa1 = wa0 + 3 * 16384;
        PINV(xa[0]); PINV(xa[1]); PINV(xa[2]); PINV(xa[3]); PINV(wa0); PINV(wa1);
        asm volatile("" :: "s"(tag));
        f32x16 a0 = zt, a1 = zt; float cw = 0.f;
        u32x4 x0[3], x1[3], wf[3], tab0, tab1;
#define GRD(n) do { wf[(n) % 3] = trfrag(WADDR((n) >> 2, ((n) & 3) * 4096), WADDR((n) >> 2, ((n) & 3) * 4096 + 512)); \
            x0[(n) % 3] = lds128(xa[(n) & 3] + ((n) >> 2) * 8192); x1[(n) % 3] = lds128(xa[(n) & 3] + ((n) >> 2) * 8192 + 4096); SB(); } while (0)
#define GCP(n) do { MF(a0, wf[(n) % 3], x0[(n) % 3], a0); cw = dot2(wf[(n) % 3][0], ONE2, cw); cw = dot2(wf[(n) % 3][1], ONE2, cw); PINV(cw); \
            MF(a1, wf[(n) % 3], x1[(n) % 3], a1); cw = dot2(wf[(n) % 3][2], ONE2, cw); cw = dot2(wf[(n) % 3][3], ONE2, cw); PINV(cw); SB(); } while (0)
#pragma unroll
        for (int n = 0; n < 34; ++n) {
            if (n < 32 && (n & 3) == 0) { asm volatile("s_barrier" ::: "memory"); if (n == 0) STAMP(1); }
            if (n == 30) asm volatile("s_barrier" ::: "memory");
            if (n == 24) { tab0 = lds128(L_TAB + l31 * 16); tab1 = lds128(L_TAB + 512 + l31 * 16); }
            if (n < 32) GRD(n);
            if (n >= 2) GCP(n - 2);
        }
        STAMP(2);
        const u32 ch = pk(cw, cw);
        const u32x4 cf = {ch, pk(cw - bfhi(ch), 0.f), 0u, 0u};
        a0 = MFMA32(cf, ((u32x4){tab0.y, tab0.z, 0u, 0u}), a0);
        a1 = MFMA32(cf, ((u32x4){tab1.y, tab1.z, 0u, 0u}), a1);
        const float r0_ = __uint_as_float(tab0.x), r1_ = __uint_as_float(tab1.x);
#pragma unroll
        for (int i = 0; i < 16; ++i) { a0[i] *= r0_; }
        u32x4 f0 = pack8(a0, 0), f1 = pack8(a0, 8);
#pragma unroll
        for (int i = 0; i < 16; ++i) { a1[i] *= r1_; }
        u32x4 f2 = pack8(a1, 0), f3 = pack8(a1, 8);
        u32 go, gs;
        if (wid < 2) { go = (2 * wid) * 4096 + lane * 16; gs = 4096; }
        else {
            const u32 vb = L_Q + (wid - 2) * 4608;
            const u32 w_ = vb + l31 * 72 + hf * 8;
            sts64(w_, (u32x2){f0.x, f0.y}); sts64(w_ + 16, (u32x2){f0.z, f0.w}); sts64(w_ + 32, (u32x2){f1.x, f1.y}); sts64(w_ + 48, (u32x2){f1.z, f1.w});
            sts64(w_ + 2304, (u32x2){f2.x, f2.y}); sts64(w_ + 2320, (u32x2){f2.z, f2.w}); sts64(w_ + 2336, (u32x2){f3.x, f3.y}); sts64(w_ + 2352, (u32x2){f3.z, f3.w});
            asm volatile("" ::: "memory");
            const u32 r_ = vb + (4 * hf + q4) * 72 + blk * 32 + p4 * 8;
            f0 = trfrag(r_, r_ + 8 * 72); f1 = trfrag(r_ + 16 * 72, r_ + 24 * 72);
            f2 = trfrag(r_ + 2304, r_ + 2304 + 8 * 72); f3 = trfrag(r_ + 2304 + 16 * 72, r_ + 2304 + 24 * 72);
            go = (2 * (wid - 2) + blk) * 4096 + 2048 + hf * 256 + (lane & 15) * 16; gs = 512;
        }
        ST128(f0, go, WS_KV + wgi * 16384); ST128(f1, go, WS_KV + wgi * 16384 + gs);
        ST128(f2, go + 1024, WS_KV + wgi * 16384); ST128(f3, go + 1024, WS_KV + wgi * 16384 + gs);
        BAR();
        sts128(L_S2 + go, f0); sts128(L_S2 + go + gs, f1); sts128(L_S2 + go + 1024, f2); sts128(L_S2 + go + gs + 1024, f3);
        WAITV(0);
        u32 cnt = 0;
        if (lane == 0) cnt = __hip_atomic_fetch_add((LAS u32*)(uintptr_t)L_CNT, 1u, __ATOMIC_RELAXED, __HIP_MEMORY_SCOPE_WORKGROUP);
        if (rfl(cnt) == 3 && lane < 8) ST128(((u32x4){tag, tag, tag, tag}), lane * 16, WS_FLAGA + wgi * 128);
        STAMP(3);
    }
    STAMP(4);
    const u32 prow = tid >> 3, ft = wid >> 1, role = tt;
    const u32x4 xres = lds128(L_X + ((g - rot) & 7) * 8192 + prow * 128 + ((l7 ^ ((prow >> 1) & 7)) << 4));
    const u32x4 wrv = *(const u32x4*)(wr + lane * 8), wrm = *(const u32x4*)(wr + g * 64 + l7 * 8);
    const bool vlane = !(lane & 16);
    const u32 voff = hf * 256 + (lane & 15) * 16;
    bool lowm[16]; u32x4 pm0, pm1;
#pragma unroll
    for (int i = 0; i < 16; ++i) lowm[i] = (i & 3) + 8 * (i >> 2) + 4 * hf <= l31;
#pragma unroll
    for (int i = 0; i < 4; ++i) { pm0[i] = (lowm[2 * i] ? 0xffffu : 0u) | (lowm[2 * i + 1] ? 0xffff0000u : 0u); pm1[i] = (lowm[8 + 2 * i] ? 0xffffu : 0u) | (lowm[9 + 2 * i] ? 0xffff0000u : 0u); }
    PINV(pm0); PINV(pm1);
    const u32 fs0 = (NFS * xcd) >> 3, fs1 = (NFS * (xcd + 1)) >> 3, fs = fs0 + rr;
    const bool isF = fs < fs1;
    const rsrc_t rwo = mkrs((const char*)wo + g * 128, 512 * 1024 - g * 128);
    u32 vwo = prow * 1024 + wsw;
    const u32 wb1 = wid * 1024;
    const u32 fimg = L_R + wid * 8192 + (wid >= 4 ? 16384u : 0u);
    const char* fsrc = ((wsw & 64) ? (const char*)wu : (const char*)wg) + (size_t)(wid * 64 + l3) * (FF * 2) + fs * 64 + (wsw & 48);
    PINV(vwo); PINV(fsrc);
    const int sU = (int)tb - 4, sF = (int)tb - (role ? 1 : 3), sG = (int)tb - 2;
    u32x4 kU, kF0, kF1, kG, vU0, vU1, vF00, vF01, vF10, vF11, vG0, vG1;
    {
        const u32 hb = WS_KV + g * 16384 + ft * 4096;
        const u32 k0 = hf * 512, kl = lane * 16;
        const u32 bU = sU >= 0 ? hb + (u32)sU * 131072 + role * 1024 : hb, bF = sF >= 0 ? hb + (u32)sF * 131072 : hb, bG = sG >= 0 ? hb + (u32)sG * 131072 + role * 1024 : hb;
        u32 aF0 = sF >= 0 ? kl : k0, aF1 = sF >= 0 ? kl + 1024 : k0, aG = sG >= 0 ? kl : k0, aU = sU >= 0 ? kl : k0;
        const u32 oU = sU >= 0 ? ONE2 : 0u, oF = sF >= 0 ? ONE2 : 0u, oG = sG >= 0 ? ONE2 : 0u;
        vU0 = vU1 = (u32x4){oU, oU, oU, oU}; vF00 = vF01 = vF10 = vF11 = (u32x4){oF, oF, oF, oF}; vG0 = vG1 = (u32x4){oG, oG, oG, oG};
        const u32 cU = sU >= 0 ? bU : WS_END, cF = sF >= 0 ? bF : WS_END, cG = sG >= 0 ? bG : WS_END;
        PINV(vU0); PINV(vU1); PINV(vF00); PINV(vF01); PINV(vF10); PINV(vF11); PINV(vG0); PINV(vG1); PINV(aF0); PINV(aF1); PINV(aG); PINV(aU);
        const int src = lane == 0 ? sU : (lane == 1 ? sF : sG);
        const u32 fo = (u32)(max(src, 0) * 8 + (int)g) * 128;
        _Pragma("nounroll") for (u32 spins = 0; spins < SPIN_CAP; ++spins) {
            u32 f = tag;
            if (lane < 3) f = LD32(fo, WS_FLAGA);
            if (__all(f == tag)) break;
            __builtin_amdgcn_s_sleep(1);
        }
        STAMP(5);
        asm volatile("buffer_load_dwordx4 %0, %4, %8, %9 offen sc1\n\tbuffer_load_dwordx4 %1, %5, %8, %9 offen sc1\n\t"
                     "buffer_load_dwordx4 %2, %6, %8, %10 offen sc1\n\tbuffer_load_dwordx4 %3, %7, %8, %11 offen sc1"
                     : "=&v"(kF0), "=&v"(kF1), "=&v"(kG), "=&v"(kU) : "v"(aF0), "v"(aF1), "v"(aG), "v"(aU), "s"(wsr), "s"(bF), "s"(bG), "s"(bU) : "memory");
        asm volatile("s_mov_b64 exec, %13\n\t"
                     "buffer_load_dwordx4 %0, %8, %9, %10 offen offset:2048 sc1\n\tbuffer_load_dwordx4 %1, %8, %9, %10 offen offset:2560 sc1\n\t"
                     "buffer_load_dwordx4 %2, %8, %9, %10 offen offset:3072 sc1\n\tbuffer_load_dwordx4 %3, %8, %9, %10 offen offset:3584 sc1\n\t"
                     "buffer_load_dwordx4 %4, %8, %9, %11 offen offset:2048 sc1\n\tbuffer_load_dwordx4 %5, %8, %9, %11 offen offset:2560 sc1\n\t"
                     "buffer_load_dwordx4 %6, %8, %9, %12 offen offset:2048 sc1\n\tbuffer_load_dwordx4 %7, %8, %9, %12 offen offset:2560 sc1\n\t"
                     "s_mov_b64 exec, -1"
                     : "+v"(vF00), "+v"(vF01), "+v"(vF10), "+v"(vF11), "+v"(vG0), "+v"(vG1), "+v"(vU0), "+v"(vU1)
                     : "v"(voff), "s"(wsr), "s"(cF), "s"(cG), "s"(cU), "s"(0x0000ffff0000ffffull) : "memory");
    }
    BAR();
    const u32x4 qown = lds128(L_Q + wid * 1024 + lane * 16), qoth = lds128(L_Q + (wid ^ 1) * 1024 + lane * 16);
    const u32x4 kL = lds128(L_S2 + ft * 4096 + role * 1024 + lane * 16);
    u32x4 vL0 = {ONE2, ONE2, ONE2, ONE2}, vL1 = vL0;
    if (vlane) { vL0 = lds128(L_S2 + ft * 4096 + 2048 + role * 1024 + voff); vL1 = lds128(L_S2 + ft * 4096 + 2048 + role * 1024 + 512 + voff); }
    BAR();
    STAMP(6);
#define WOD(c) dma1<L_X + (c) * 8192>(wb1, vwo, rwo, XOFF(c) * 512)
#define FFD(c) do { if (isF) asm volatile("s_add_u32 m0, %0, %2\n\ts_nop 0\n\tglobal_load_lds_dwordx4 %1, off" :: "s"(fimg), "v"(fsrc + (size_t)(c) * 8 * (FF * 2)), "i"((c) * 1024) : "scc", "memory"); } while (0)
    f32x16 oacc; float mrow;
    {
        f32x16 sa0, sa1, sm;
        float mo, mw;
        asm volatile("s_waitcnt vmcnt(8)" : "+v"(kF0), "+v"(kF1), "+v"(kG), "+v"(kU) :: "memory");
        MF(sa0, kF0, qoth, zt); MF(sa1, kF0, qown, zt);
        MF(sm, kF1, qoth, zt); mo = max16(sa0, -1e30f); PINV(mo);
        MF(sa0, kF1, qown, zt); mw = max16(sa1, -1e30f); PINV(mw);
        MF(sa1, kG, qoth, zt); mo = max16(sm, mo); PINV(mo);
        MF(sm, kG, qown, zt); mw = max16(sa0, mw); PINV(mw);
        u32x4 kX = role ? kU : kL;
        MF(sa0, kX, qoth, zt); mo = max16(sa1, mo); PINV(mo);
        MF(sa1, kU, qown, zt); mw = max16(sm, mw); PINV(mw);
        MF(sm, kL, qown, zt); mo = max16(sa0, mo); mo = xhalf_max(mo);
        f32x16 nmo;
#pragma unroll
        for (int i = 0; i < 16; ++i) nmo[i] = -mo;
        PINV(nmo);
        MF(sa0, kF0, qoth, nmo);
#pragma unroll
        for (int i = 0; i < 16; ++i) sm[i] = lowm[i] ? sm[i] : sa1[i];
        mw = max16(sm, mw); mw = xhalf_max(mw); PINV(mw); PINV(sm); SB();
        asm volatile("s_waitcnt vmcnt(0)" : "+v"(vF00), "+v"(vF01), "+v"(vF10), "+v"(vF11), "+v"(vG0), "+v"(vG1), "+v"(vU0), "+v"(vU1) :: "memory");
        STAMP(7);
#define EXPH(S, o) do { _Pragma("unroll") for (int i = 0; i < 8; ++i) S[(o) + i] = __builtin_amdgcn_exp2f(S[(o) + i]); } while (0)
#define TILE(S, V0, V1, OIN, NEXT, EXTRA) do { NEXT; EXPH(S, 0); u32x4 p_ = pack8(S, 0); MF(oacc, V0, p_, OIN); EXTRA; EXPH(S, 8); p_ = pack8(S, 8); MF(oacc, V1, p_, oacc); } while (0)
        TILE(sa0, vF00, vF01, zt, MF(sa1, kF1, qoth, nmo), WOD(0); WOD(1));
        TILE(sa1, vF10, vF11, oacc, MF(sa0, kG, qoth, nmo), WOD(2); WOD(3));
        TILE(sa0, vG0, vG1, oacc, MF(sa1, kX, qoth, nmo), WOD(4); WOD(5));
        const u32x4 vX0 = role ? vU0 : vL0, vX1 = role ? vU1 : vL1;
#pragma unroll
        for (int i = 0; i < 16; ++i) nmo[i] = -mw;
        PINV(nmo);
        TILE(sa1, vX0, vX1, oacc, MF(sa0, kF0, qown, nmo), WOD(6); WOD(7));
        {
#pragma unroll
            for (int i = 0; i < 16; ++i) sm[i] = __builtin_amdgcn_exp2f(sm[i] - mw);
            const u32x4 p0 = pack8(sm, 0), p1 = pack8(sm, 8);
            u32x4 l0 = p0 & pm0, l1 = p1 & pm1, u0 = p0 ^ l0, u1 = p1 ^ l1;
            PINV(l0); PINV(l1); PINV(u0); PINV(u1);
            SB();
            const u32 e = L_S2 + wid * 2048 + lane * 16;
            stsf4(e, (f32x4){oacc[0], oacc[1], oacc[2], oacc[3]}); stsf4(e + 1024, (f32x4){oacc[4], oacc[5], oacc[6], oacc[7]});
            sts64(L_M + wid * 512 + lane * 8, (u32x2){__float_as_uint(mo), __float_as_uint(oacc[8])});
            MF(sm, vL0, l0, zt); FFD(0); MF(sm, vL1, l1, sm); FFD(1);
            MF(sa1, kF1, qown, nmo);
            EXPH(sa0, 0); u32x4 p_ = pack8(sa0, 0); PINV(p_);
            MF(sm, vU0, u0, sm);
            EXPH(sa0, 8); u32x4 q_ = pack8(sa0, 8); PINV(q_);
            MF(sm, vU1, u1, sm);
            FFD(2); MF(oacc, vF00, p_, sm); FFD(3); MF(oacc, vF01, q_, oacc);
        }
        TILE(sa1, vF10, vF11, oacc, MF(sa0, kG, qown, nmo), FFD(4); FFD(5));
        TILE(sa0, vG0, vG1, oacc, FFD(6), FFD(7));
        mrow = mw;
    }
    BAR();
    {
        const u32 e = L_S2 + (wid ^ 1) * 2048 + lane * 16;
        const f32x4 o0 = ldsf4(e), o1 = ldsf4(e + 1024);
        const u32x2 ml = lds64(L_M + (wid ^ 1) * 512 + lane * 8);
        const float m2 = __uint_as_float(ml.x), mx = fmaxf(mrow, m2);
        const float a = __builtin_amdgcn_exp2f(mrow - mx), c = __builtin_amdgcn_exp2f(m2 - mx);
        const float inv = __builtin_amdgcn_rcpf(oacc[8] * a + __uint_as_float(ml.y) * c);
        const float fa = a * inv, fc = c * inv;
        const u32x4 of = {pk(oacc[0] * fa + o0[0] * fc, oacc[1] * fa + o0[1] * fc), pk(oacc[2] * fa + o0[2] * fc, oacc[3] * fa + o0[3] * fc),
                          pk(oacc[4] * fa + o1[0] * fc, oacc[5] * fa + o1[1] * fc), pk(oacc[6] * fa + o1[2] * fc, oacc[7] * fa + o1[3] * fc)};
        ST128(of, lane * 16, WS_O + ((tb * 2 + role) * 32 + g * 4 + ft) * 1024);
    }
    STAMP(8);
    waitbar<0>();
    if (tid < 8) ST128(((u32x4){tag, tag, tag, tag}), tid * 16, WS_FLAGB + wgi * 128);
    STAMP(9);

    const u32 gq = lane >> 4;
    u32x4 gf[2][4];
#define GFREAD() do { const u32 fa_ = fimg + (16 * gq + q4) * 128 + ((p4 * 8) ^ ((q4 >> 1) << 6)); \
        _Pragma("unroll") for (int s = 0; s < 2; ++s) _Pragma("unroll") for (int t = 0; t < 4; ++t) { \
            const u32 a_ = (fa_ ^ ((t & 2) << 5)) + (t & 1) * 32 + s * 1024; gf[s][t] = trfrag(a_, a_ + 512); } } while (0)
    if (wid < 4) {
        u32x4 wof[16][2];
        u32 b0 = L_X + ft * 32768 + (4 * hf + q4) * 128 + trc, b1 = b0 ^ 64;
        PINV(b0); PINV(b1);
#pragma unroll
        for (int i = 0; i < 16; ++i) {
            wof[i][0] = trfrag(b0 + i * 2048, b0 + i * 2048 + 1024);
            wof[i][1] = trfrag(b1 + i * 2048, b1 + i * 2048 + 1024);
            PINV(wof[i][0]); PINV(wof[i][1]);
        }
        u32 pb = (ft ? L_M : L_S2) + (tt * 32 + l31) * 256 + ((hf ^ l7) << 4);
        u32 pb1 = pb ^ 32, pb2 = pb ^ 64, pb3 = pb ^ 96;
        PINV(pb); PINV(pb1); PINV(pb2); PINV(pb3);
        const u32 fo = (tb * 8 + ((ft * 4 + (lane & 3) + rot) & 7)) * 128;
        _Pragma("nounroll") for (u32 spins = 0; spins < SPIN_CAP; ++spins) {
            u32 f = tag;
            if (lane < 4) f = LD32(fo, WS_FLAGB);
            if (__all(f == tag)) break;
            __builtin_amdgcn_s_sleep(1);
        }
        __builtin_amdgcn_fence(__ATOMIC_ACQUIRE, "wavefront");
        STAMP(10);
        u32x4 of[16];
#pragma unroll
        for (int i = 0; i < 16; ++i) of[i] = LD128(lane * 16 + (i & 3) * 1024, WS_O + (tb * 2 + tt) * 32768 + ((ft * 4 + (i >> 2) + rot) & 7) * 4096);
        SB();
        f32x16 a0 = zt, a1 = zt;
#pragma unroll
        for (int i = 0; i < 16; ++i) { MF(a0, wof[i][0], of[i], a0); MF(a1, wof[i][1], of[i], a1); }
        stsf4(pb, (f32x4){a0[0], a0[1], a0[2], a0[3]}); stsf4(pb1, (f32x4){a0[4], a0[5], a0[6], a0[7]});
        stsf4(pb2, (f32x4){a0[8], a0[9], a0[10], a0[11]}); stsf4(pb3, (f32x4){a0[12], a0[13], a0[14], a0[15]});
        stsf4(pb + 128, (f32x4){a1[0], a1[1], a1[2], a1[3]}); stsf4(pb1 + 128, (f32x4){a1[4], a1[5], a1[6], a1[7]});
        stsf4(pb2 + 128, (f32x4){a1[8], a1[9], a1[10], a1[11]}); stsf4(pb3 + 128, (f32x4){a1[12], a1[13], a1[14], a1[15]});
    } else if (isF) {
        GFREAD();
        asm volatile("s_waitcnt lgkmcnt(0)" ::: "memory");
        const rsrc_t rwd = mkrs((const char*)wd + (size_t)fs * 32768, 32768);
        const u32 vo = l3 * 1024 + wsw;
#pragma unroll
        for (int c = 0; c < 8; ++c) dma1<0>(fimg + c * 1024, vo, rwd, (c & 3) * 8192 + ((wid - 4) * 2 + (c >> 2)) * 128);
    }
    waitbar<0>();
    u32x4 hp;
    {
        const u32 ra = prow * 256 + (((2 * l7) ^ (prow & 7)) << 4);
        const f32x4 r0 = ldsf4(L_S2 + ra) + ldsf4(L_M + ra), r1 = ldsf4(L_S2 + (ra ^ 16)) + ldsf4(L_M + (ra ^ 16));
        hp = (u32x4){pk(r0[0] + bflo(xres.x), r0[1] + bfhi(xres.x)), pk(r0[2] + bflo(xres.y), r0[3] + bfhi(xres.y)),
                     pk(r1[0] + bflo(xres.z), r1[1] + bfhi(xres.z)), pk(r1[2] + bflo(xres.w), r1[3] + bfhi(xres.w))};
        float s1 = 0.f, s2 = 0.f, s3 = 0.f;
#pragma unroll
        for (int e = 0; e < 4; ++e) { s1 = dot2(hp[e], ONE2, s1); s2 = dot2(hp[e], hp[e], s2); s3 = dot2(hp[e], wrm[e], s3); }
        s1 = sum8(s1); s2 = sum8(s2); s3 = sum8(s3);
        if (l7 == 0) ST128(((u32x4){__float_as_uint(s1), __float_as_uint(s2), __float_as_uint(s3), tag}), prow * 16, WS_STAT + wgi * 1024);
    }
    STAMP(11);
    constexpr u32 L_ST = L_X, L_WIN = L_X + 512, L_STASH = L_X + 1024, L_RED = L_X + 4096;
    if (wid == 7) {
        float wsum;
        { float s = dot2(wrv[0], ONE2, 0.f); s = dot2(wrv[1], ONE2, s); s = dot2(wrv[2], ONE2, s); s = dot2(wrv[3], ONE2, s);
          s = sum8(s); s = dppadd<0x140>(s);
          wsum = (__uint_as_float(__builtin_amdgcn_readlane(__float_as_uint(s), 0)) + __uint_as_float(__builtin_amdgcn_readlane(__float_as_uint(s), 16)))
               + (__uint_as_float(__builtin_amdgcn_readlane(__float_as_uint(s), 32)) + __uint_as_float(__builtin_amdgcn_readlane(__float_as_uint(s), 48))); }
        __builtin_amdgcn_s_sleep(3);
        float s1, s2, s3;
        _Pragma("nounroll") for (u32 spins = 0; spins < SPIN_CAP; ++spins) {
            u32x4 a[8];
#pragma unroll
            for (int k = 0; k < 8; ++k) a[k] = LD128(lane * 16 + (k & 3) * 1024, WS_STAT + tb * 8192 + (k >> 2) * 4096);
            u32 bad = 0; s1 = s2 = s3 = 0.f;
#pragma unroll
            for (int k = 0; k < 8; ++k) { bad |= a[k].w ^ tag; s1 += __uint_as_float(a[k].x); s2 += __uint_as_float(a[k].y); s3 += __uint_as_float(a[k].z); }
            if (__all(bad == 0)) break;
            __builtin_amdgcn_s_sleep(1);
        }
        const float mu = s1 * (1.f / 512.f);
        const float rs = __builtin_amdgcn_rsqf(fmaxf(s2 * (1.f / 512.f) - mu * mu, 0.f) + 1e-5f);
        const u32 key = (fkey((s3 - mu * wsum) * rs) & ~63u) | (63u - lane);
        sts64(L_ST + lane * 8, (u32x2){__float_as_uint(rs), __float_as_uint(-mu * rs)});
        const u32 k1 = wave_umax(key), k2 = wave_umax(key == k1 ? 0u : key);
        if (g == 0 && lane < 8) ST128(((u32x4){k1, tag, k2, tag}), lane * 16, WS_CAND + tb * 128);
        if (lane == 0) sts64(L_WIN, (u32x2){63u - (k1 & 63u), 63u - (k2 & 63u)});
    } else if (isF && wid < 4) GFREAD();
    BAR();
    STAMP(12);
    u32 c1, c2;
    u32x4 ov;
    {
        const u32x2 st = lds64(L_ST + prow * 8), cn = lds64(L_WIN);
        c1 = cn.x; c2 = cn.y;
        const float rs = __uint_as_float(st.x), nb = __uint_as_float(st.y);
        u32x4 hn;
#pragma unroll
        for (int e = 0; e < 4; ++e) hn[e] = pk(bflo(hp[e]) * rs + nb, bfhi(hp[e]) * rs + nb);
        if (prow == c1 || prow == c2) {
            const u32 ci = prow == c1 ? 0u : 1u;
            const u32 ro = ci * 2048 + l7 * 32;
            ST128(((u32x4){hn[0], tag, hn[1], tag}), ro, WS_ROWS + tb * 4096 + g * 256);
            ST128(((u32x4){hn[2], tag, hn[3], tag}), ro + 16, WS_ROWS + tb * 4096 + g * 256);
            sts128(L_STASH + ci * 256 + l7 * 16, hp); sts128(L_STASH + ci * 256 + 128 + l7 * 16, hn);
        }
#pragma unroll
        for (int e = 0; e < 4; ++e) ov[e] = pk(bflo(hp[e]) + bflo(hn[e]), bfhi(hp[e]) + bfhi(hn[e]));
    }
    u16* const orow = out + (size_t)(tb * 64 + prow) * 512 + g * 64 + l7 * 8;
    if (wid == 7) {
        if (!isF) __builtin_amdgcn_s_sleep(24);
        u32 ka = 0, kb = 0;
        _Pragma("nounroll") for (u32 spins = 0; spins < SPIN_CAP; ++spins) {
            const u32x4 c = LD128(l31 * 128, WS_CAND);
            ka = c.x; kb = c.z;
            if (__all(((c.y ^ tag) | (c.w ^ tag)) == 0)) break;
            if (isF) __builtin_amdgcn_s_sleep(1); else __builtin_amdgcn_s_sleep(8);
        }
        const u32 key = hf ? kb : ka;
        const u32 k1 = wave_umax(key);
        const u32 l1 = (u32)__builtin_ctzll(__ballot(key == k1));
        const u32 key2 = lane == l1 ? 0u : key;
        const u32 k2 = wave_umax(key2);
        const u32 l2 = (u32)__builtin_ctzll(__ballot(key2 == k2 && lane != l1));
        if (lane == 0) sts64(L_WIN + 8, (u32x2){((l1 & 31u) * 2 + (l1 >> 5)) | (k1 & ~63u), ((l2 & 31u) * 2 + (l2 >> 5)) | (k2 & ~63u)});
    }
    if (prow != c1 && prow != c2) *(u32x4*)orow = ov;
    u32x4 wdf[4];
    if (isF) {
        const u32 db = L_R + 49152 + wid * 4096 + (8 * gq + q4) * 128 + ((p4 * 8) ^ ((q4 >> 1) << 6));
#pragma unroll
        for (int t = 0; t < 4; ++t) { const u32 a_ = (db ^ ((t & 2) << 5)) + (t & 1) * 32; wdf[t] = trfrag(a_, a_ + 512); }
    }
    BAR();
    const u32x2 wn = lds64(L_WIN + 8);
    const u32 w1 = rfl(wn.x), w2 = rfl(wn.y);
    STAMP(13);
    if (isF) {
        const u32 slot = ((lane & 1) ? w2 : w1) & 63u;
        const u32 ro = slot * 2048 + gq * 64;
        u32x4 af0 = {0, 0, 0, 0}, af1 = af0;
        _Pragma("nounroll") for (u32 spins = 0; spins < SPIN_CAP; ++spins) {
            u32 bad = 0;
            if ((lane & 14) == 0) {
                const u32x4 a0 = LD128(ro, WS_ROWS + wid * 256), a1 = LD128(ro + 16, WS_ROWS + wid * 256), a2 = LD128(ro + 32, WS_ROWS + wid * 256), a3 = LD128(ro + 48, WS_ROWS + wid * 256);
                af0 = (u32x4){a0.x, a0.z, a1.x, a1.z}; af1 = (u32x4){a2.x, a2.z, a3.x, a3.z};
                bad = (a0.y ^ tag) | (a0.w ^ tag) | (a1.y ^ tag) | (a1.w ^ tag) | (a2.y ^ tag) | (a2.w ^ tag) | (a3.y ^ tag) | (a3.w ^ tag);
            }
            if (__all(bad == 0)) break;
        }
        f32x4 d[4];
#pragma unroll
        for (int t = 0; t < 4; ++t) { d[t] = MFMA16(af0, gf[0][t], ((f32x4){0.f, 0.f, 0.f, 0.f})); d[t] = MFMA16(af1, gf[1][t], d[t]); }
        if (lane < 16) {
#pragma unroll
            for (int t = 0; t < 4; ++t) { stsf(L_RED + t * 1024 + lane * 64 + wid * 4, d[t][0]); stsf(L_RED + t * 1024 + lane * 64 + 32 + wid * 4, d[t][1]); }
        }
        BAR();
        const u32 ga = L_RED + l31 * 64 + hf * 32;
        const f32x4 g0 = ldsf4(ga) + ldsf4(ga + 16), u0 = ldsf4(ga + 2048) + ldsf4(ga + 2048 + 16);
        const float gv = rbf((g0[0] + g0[1]) + (g0[2] + g0[3])), uv = rbf((u0[0] + u0[1]) + (u0[2] + u0[3]));
        const float sg = rbf(gv * __builtin_amdgcn_rcpf(1.f + __builtin_amdgcn_exp2f(-1.4426950408889634f * gv)));
        const u32 ab = L_RED + 4096 + wid * 128;
        *(LAS u16*)(uintptr_t)(ab + lane * 2) = (u16)pk(sg * uv, 0.f);
        asm volatile("" ::: "memory");
        const u32x4 a2 = lds128(ab + (lane & 1) * 64 + gq * 16);
#pragma unroll
        for (int t = 0; t < 4; ++t) d[t] = MFMA16(a2, wdf[t], ((f32x4){0.f, 0.f, 0.f, 0.f}));
        if (lane < 32) {
#pragma unroll
            for (int t = 0; t < 4; ++t) ST64(((u32x2){__float_as_uint(blk ? d[t][1] : d[t][0]), tag}), blk * 4096 + (lane & 15) * 8 + t * 128, WS_Y + fs * 8192 + wid * 512);
        }
    }
    STAMP(14);
    const bool own1 = ((w1 & 63u) >> 1) == tb, own2 = ((w2 & 63u) >> 1) == tb;
    const u32 t1 = own1 ? ((w1 & 1u) ? c2 : c1) : 64u, t2 = own2 ? ((w2 & 1u) ? c2 : c1) : 64u;
    if ((prow == c1 || prow == c2) && prow != t1 && prow != t2) *(u32x4*)orow = ov;
    if (own1 || own2) {
#pragma unroll 1
        for (u32 wsel = 0; wsel < 2; ++wsel) {
            if (!(wsel ? own2 : own1)) continue;
            const u32 wk = wsel ? w2 : w1;
            float s;
            _Pragma("nounroll") for (u32 spins = 0; spins < SPIN_CAP; ++spins) {
                u32 bad = 0; s = 0.f;
                u32x2 y[6];
#pragma unroll
                for (int k = 0; k < 6; ++k) { const u32 src = min(wid + 8u * k, (u32)NFS - 1u); y[k] = LD64(lane * 8, WS_Y + src * 8192 + wsel * 4096 + g * 512); }
#pragma unroll
                for (int k = 0; k < 6; ++k) { bad |= y[k].y ^ tag; s += (wid + 8u * k < (u32)NFS) ? __uint_as_float(y[k].x) : 0.f; }
                if (__all(bad == 0)) break;
                __builtin_amdgcn_s_sleep(1);
            }
            stsf(L_RED + 8192 + wsel * 2048 + wid * 256 + lane * 4, s);
            BAR();
            if (wid == 0) {
                float y = 0.f;
#pragma unroll
                for (int k = 0; k < 8; ++k) y += ldsf(L_RED + 8192 + wsel * 2048 + k * 256 + lane * 4);
                const float rv = rbf(fkey_inv(wk & ~63u));
                const float sg = rbf(__builtin_amdgcn_rcpf(1.f + __builtin_amdgcn_exp2f(-1.4426950408889634f * rv)));
                const float hh = bflo(*(const LAS u16*)(uintptr_t)(L_STASH + (wk & 1u) * 256 + lane * 2));
                const float hq = bflo(*(const LAS u16*)(uintptr_t)(L_STASH + (wk & 1u) * 256 + 128 + lane * 2));
                const float res = hh + rbf(hq + rbf(rbf(y) * sg));
                const u32 tok = wsel ? t2 : t1;
                out[(size_t)(tb * 64 + tok) * 512 + g * 64 + lane] = (u16)pk(res, 0.f);
                if (wsel == 0 && g == 0 && lane == 0) __builtin_amdgcn_raw_buffer_store_b32(tag, wsr, 0, WS_EPOCH, SC1);
            }
        }
    }
    STAMP(15);
}

extern "C" void kernel_launch(void* const* d_in, const int* in_sizes, int n_in, void* d_out, int out_size, void* d_ws, size_t ws_size, hipStream_t stream) {
    (void)in_sizes; (void)n_in; (void)out_size; (void)ws_size;
    (void)hipFuncSetAttribute((const void*)fused_block, hipFuncAttributeMaxDynamicSharedMemorySize, LDS_BYTES);
    hipLaunchKernelGGL(fused_block, dim3(256), dim3(512), LDS_BYTES, stream,
                       (const u16*)d_in[0], (const u16*)d_in[1], (const u16*)d_in[2], (const u16*)d_in[4], (const u16*)d_in[5],
                       (const u16*)d_in[6], (const u16*)d_in[7], (u16*)d_out, (const u32*)d_ws, (char*)d_ws);
}
```

```cpp
#include <hip/hip_runtime.h>
#include <stdint.h>

typedef unsigned short u16;
typedef unsigned u32;
typedef unsigned long long u64;
typedef __bf16 bf16x2 __attribute__((ext_vector_type(2)));
typedef __bf16 bf16x8 __attribute__((ext_vector_type(8)));
typedef short s16x4 __attribute__((ext_vector_type(4)));
typedef float f32x2 __attribute__((ext_vector_type(2)));
typedef float f32x4 __attribute__((ext_vector_type(4)));
typedef float f32x16 __attribute__((ext_vector_type(16)));
typedef u32 u32x2 __attribute__((ext_vector_type(2)));
typedef u32 u32x4 __attribute__((ext_vector_type(4)));
#define LAS __attribute__((address_space(3)))
#define DI __device__ __forceinline__
#define SC1 16

constexpr int FF = 1376;
constexpr int DSLEEP = 28, BSLEEP = 6;
constexpr int DG = 3;
constexpr u32 CODE_LINES = 176;
constexpr int NFS = 43;
constexpr u32 CHK = 0x9e3779b9u;
constexpr u32 SPIN_CAP = 1u << 19;
constexpr u32 WS_EPOCH = 0;
constexpr u32 WS_FLAGA = 0x1000;
constexpr u32 WS_FLAGB = WS_FLAGA + 1024 * 128;
constexpr u32 WS_CAND = WS_FLAGB + 256 * 128;
constexpr u32 WS_ROWS = WS_CAND + 8 * 4096;
constexpr u32 WS_STAT = WS_ROWS + 32 * 4096;
constexpr u32 WS_Y = WS_STAT + 256 * 2048;
constexpr u32 WS_TAB = WS_Y + NFS * 8192;
constexpr u32 WS_KV = 0x200000;
constexpr u32 WS_O = WS_KV + 256 * 16384;
constexpr u32 WS_END = WS_O + 2048 * 1024;
static_assert(WS_TAB + 32768 <= WS_KV, "ws map");
constexpr u32 L_X = 0, L_R = 65536, LDS_BYTES = 163840;
constexpr u32 L_M = L_R;
constexpr u32 L_S2 = L_R + 16384;
constexpr u32 L_F = L_R + 32768;
constexpr u32 L_EX = L_F;
constexpr u32 L_Q = L_M + 2048;

typedef __amdgpu_buffer_rsrc_t rsrc_t;
template <int V> struct IC { static constexpr int value = V; };
template <int I, int N, class F> DI void sfor(F&& f) { if constexpr (I < N) { f(IC<I>{}); sfor<I + 1, N>(f); } }
DI u32 rfl(u32 v) { return __builtin_amdgcn_readfirstlane(v); }
DI rsrc_t mkrs(const void* p, u32 n) { return __builtin_amdgcn_make_buffer_rsrc((void*)p, 0, n, 0x00020000); }
template <u32 IMM> DI void dma1(u32 wb, u32 voff, rsrc_t rs, u32 soff) {
    asm volatile("s_add_u32 m0, %0, %4\n\ts_nop 0\n\tbuffer_load_dwordx4 %1, %2, %3 offen lds" :: "s"(wb), "v"(voff), "s"(rs), "s"(soff), "i"(IMM) : "scc", "memory");
}
template <u32 IMM> DI void dma2(u32 wb, u32 voff, rsrc_t rs, u32 soff) {
    asm volatile("s_add_u32 m0, %0, %4\n\ts_nop 0\n\tbuffer_load_dwordx4 %1, %2, %3 offen lds\n\tbuffer_load_dwordx4 %1, %2, %3 offen offset:1024 lds"
                 :: "s"(wb), "v"(voff), "s"(rs), "s"(soff), "i"(IMM) : "scc", "memory");
}
#define MEMF() asm volatile("" ::: "memory")
#define SB() __builtin_amdgcn_sched_barrier(0)
#define PINV(x) asm volatile("" : "+v"(x))
#define PINS(x) asm volatile("" : "+s"(x))
#define BAR() asm volatile("s_waitcnt lgkmcnt(0)\n\ts_barrier" ::: "memory")
template <int N> DI void waitbar() { asm volatile("s_waitcnt vmcnt(%0) lgkmcnt(0)\n\ts_barrier" :: "i"(N) : "memory"); }
#define WAITV(n) asm volatile("s_waitcnt vmcnt(" #n ")" ::: "memory")
#define MFMA32(a, b, c) __builtin_amdgcn_mfma_f32_32x32x16_bf16(__builtin_bit_cast(bf16x8, a), __builtin_bit_cast(bf16x8, b), c, 0, 0, 0)
#define MFMA16(a, b, c) __builtin_amdgcn_mfma_f32_16x16x32_bf16(__builtin_bit_cast(bf16x8, a), __builtin_bit_cast(bf16x8, b), c, 0, 0, 0)
#define LD128(off, soff) __builtin_amdgcn_raw_buffer_load_b128(wsr, off, soff, SC1)
#define LD64(off, soff) __builtin_amdgcn_raw_buffer_load_b64(wsr, off, soff, SC1)
#define LD32(off, soff) __builtin_amdgcn_raw_buffer_load_b32(wsr, off, soff, SC1)
#define ST128(v, off, soff) asm volatile("buffer_store_dwordx4 %0, %1, %2, %3 offen sc1\n\ts_nop 1" :: "v"(v), "v"(off), "s"(wsr), "s"(soff) : "memory")
DI u32x4 lds128(u32 a) { return *(const LAS u32x4*)(uintptr_t)a; }
DI u32x2 lds64(u32 a) { return *(const LAS u32x2*)(uintptr_t)a; }
DI f32x4 ldsf4(u32 a) { return *(const LAS f32x4*)(uintptr_t)a; }
DI void sts128(u32 a, u32x4 v) { *(LAS u32x4*)(uintptr_t)a = v; }
DI void sts64(u32 a, u32x2 v) { *(LAS u32x2*)(uintptr_t)a = v; }
DI void stsf4(u32 a, f32x4 v) { *(LAS f32x4*)(uintptr_t)a = v; }
DI void stsf(u32 a, float v) { *(LAS float*)(uintptr_t)a = v; }
DI float ldsf(u32 a) { return *(const LAS float*)(uintptr_t)a; }
DI u32x2 ldstr(u32 a) { return __builtin_bit_cast(u32x2, __builtin_amdgcn_ds_read_tr16_b64_v4i16((LAS s16x4*)(uintptr_t)a)); }
DI u32x4 trfrag(u32 a0, u32 a1) { u32x2 lo = ldstr(a0), hi = ldstr(a1); return (u32x4){lo.x, lo.y, hi.x, hi.y}; }
DI u32 pk(float a, float b) { f32x2 v = {a, b}; return __builtin_bit_cast(u32, __builtin_convertvector(v, bf16x2)); }
DI float dot2(u32 a, u32 b, float c) { return __builtin_amdgcn_fdot2_f32_bf16(__builtin_bit_cast(bf16x2, a), __builtin_bit_cast(bf16x2, b), c, false); }
DI float bflo(u32 v) { return __uint_as_float(v << 16); }
DI float bfhi(u32 v) { return __uint_as_float(v & 0xffff0000u); }
DI float rbf(float v) { return bfhi(pk(v, v)); }
DI float rstd_of(float var) { return __builtin_amdgcn_rcpf(rbf(__builtin_amdgcn_sqrtf(rbf(rbf(fmaxf(var, 0.f)) + 1e-5f)))); }
DI float xhalf_max(float v) { auto r = __builtin_amdgcn_permlane32_swap(__float_as_uint(v), __float_as_uint(v), false, false); return fmaxf(__uint_as_float(r[0]), __uint_as_float(r[1])); }
constexpr u32 ONE2 = 0x3f803f80u;
DI u32x4 pack8(const f32x16& v, int o) { return (u32x4){pk(v[o], v[o + 1]), pk(v[o + 2], v[o + 3]), pk(v[o + 4], v[o + 5]), pk(v[o + 6], v[o + 7])}; }
DI float max16(const f32x16& s, float m) {
#pragma unroll
    for (int r = 0; r < 16; r += 2) m = __builtin_fmaxf(__builtin_fmaxf(m, s[r]), s[r + 1]);
    return m;
}
template <int CTRL> DI float dppadd(float v) { return v + __int_as_float(__builtin_amdgcn_update_dpp(0, __float_as_int(v), CTRL, 0xf, 0xf, true)); }
DI float sum64(float v) {
    v = dppadd<0x111>(v); v = dppadd<0x112>(v); v = dppadd<0x114>(v); v = dppadd<0x118>(v);
    v += __int_as_float(__builtin_amdgcn_update_dpp(0, __float_as_int(v), 0x142, 0xa, 0xf, false));
    v += __int_as_float(__builtin_amdgcn_update_dpp(0, __float_as_int(v), 0x143, 0xc, 0xf, false));
    return v;
}
DI float sum32(float v) {
    v = dppadd<0x111>(v); v = dppadd<0x112>(v); v = dppadd<0x114>(v); v = dppadd<0x118>(v);
    return v + __int_as_float(__builtin_amdgcn_update_dpp(0, __float_as_int(v), 0x142, 0xa, 0xf, false));
}
DI float sum8(float v) { v = dppadd<0xB1>(v); v = dppadd<0x4E>(v); return dppadd<0x141>(v); }
template <int CTRL> DI u32 dppmax(u32 v) { return max(v, (u32)__builtin_amdgcn_update_dpp(0, (int)v, CTRL, 0xf, 0xf, true)); }
DI u32 wave_umax(u32 v) {
    v = dppmax<0x111>(v); v = dppmax<0x112>(v); v = dppmax<0x114>(v); v = dppmax<0x118>(v); v = dppmax<0x142>(v); v = dppmax<0x143>(v);
    return __builtin_amdgcn_readlane(v, 63);
}
DI u32 fkey(float f) { u32 u = __float_as_uint(f); return u ^ ((u32)((int)u >> 31) | 0x80000000u); }
DI float fkey_inv(u32 k) { u32 u = (k & 0x80000000u) ? (k ^ 0x80000000u) : ~k; return __uint_as_float(u); }

__global__ __launch_bounds__(512, 2) void fused_block(const u16* __restrict__ x, const u16* __restrict__ wqkv, const u16* __restrict__ wo,
                                                      const u16* __restrict__ wr, const u16* __restrict__ wg, const u16* __restrict__ wu,
                                                      const u16* __restrict__ wd, u16* __restrict__ out, const u32* __restrict__ epoch_rd, char* ws) {
    const u32 b = blockIdx.x, tid = threadIdx.x, lane = tid & 63, wid = rfl(tid >> 6);
    const u32 xcd = b & 7, rr = b >> 3;
    const u32 tb = (xcd & 3) * 8 + (rr & 7), g = (xcd >> 2) * 4 + (rr >> 3), rot = (rr & 7) + 2 * (rr >> 3);
    const u32 wgi = tb * 8 + g;
    const u32 l31 = lane & 31, hf = lane >> 5, l7 = lane & 7, l3 = lane >> 3;
    const u32 q4 = (lane & 15) >> 2, p4 = lane & 3, blk = (lane >> 4) & 1;
    const u32 wsw = (l7 ^ (blk << 2)) << 4;
    const u32 trc = (blk * 32 + p4 * 8) ^ ((q4 >> 1) << 6);
    const rsrc_t rw = mkrs((const char*)wqkv + g * 128, 512 * 3072 - g * 128);
    const rsrc_t wsr = mkrs(ws, WS_END);
#define OUTST() asm volatile("global_store_dwordx4 %0, %1, off sc1\n\ts_nop 1" :: "v"(orow), "v"(ov) : "memory")
#define XOFF(c) ((((c) + rot) & 7) * 128)
#define MF(D, A, B, C) do { SB(); D = MFMA32(A, B, C); PINV(D); SB(); } while (0)
    const u32 hw = wid & 3, tt = wid & 1, ft = wid >> 1, role = wid >> 2;
    const bool helper = wid >= 4;
    const rsrc_t rx = mkrs((const char*)x + (size_t)tb * 65536, 65536);
    const u32 r0 = hw * 16 + l3;
    const u32 vx0 = r0 * 1024 + ((l7 ^ ((r0 >> 1) & 7)) << 4), vx1 = (vx0 + 8192) ^ 64;
    const u32 vw0 = r0 * 3072 + wsw, vw1 = vw0 + 8 * 3072;
    const u32 wbx = hw * 2048, wbw = hw * 4096;
#define XD(c) do { dma1<L_X + (c) * 8192>(wbx, vx0, rx, XOFF(c)); dma1<L_X + (c) * 8192 + 1024>(wbx, vx1, rx, XOFF(c)); } while (0)
#define WD(s) do { \
        if ((s) < 8) { dma2<L_R + ((s) % 6) * 16384>(wbw, vw0, rw, XOFF(s) * 1536 + 1024); dma2<L_R + ((s) % 6) * 16384 + 2048>(wbw, vw1, rw, XOFF(s) * 1536 + 1024); } \
        else { dma1<L_R + ((s) % 6) * 16384>(wbw, vw0, rw, XOFF(2 * ((s) - 8)) * 1536); dma1<L_R + ((s) % 6) * 16384 + 2048>(wbw, vw1, rw, XOFF(2 * ((s) - 8)) * 1536); \
               dma1<L_R + ((s) % 6) * 16384 + 1024>(wbw, vw0, rw, XOFF(2 * ((s) - 8) + 1) * 1536); dma1<L_R + ((s) % 6) * 16384 + 3072>(wbw, vw1, rw, XOFF(2 * ((s) - 8) + 1) * 1536); } \
    } while (0)
    u32 pfd = 0;
    if (wid == 7) {
        u64 pc; asm volatile("s_getpc_b64 %0" : "=s"(pc));
        const u32 line = rr * 8 + l7;
        if (lane < 8 && line < CODE_LINES) asm volatile("global_load_dword %0, %1, %2" : "=v"(pfd) : "v"(line * 128), "s"(pc & ~127ull) : "memory");
    }
    u32x4 xr0, xr1;
    if (helper) {
        asm volatile("buffer_load_dwordx4 %0, %2, %3, %4 offen\n\tbuffer_load_dwordx4 %1, %2, %3, %4 offen offset:1024" : "=&v"(xr0), "=&v"(xr1) : "v"(lane * 16), "s"(rx), "s"((g * 8 + hw * 2) * 1024) : "memory");
        XD(0);
    } else { asm volatile("" : "=v"(xr0), "=v"(xr1)); WD(0); }
    SB();
    u32 tag = epoch_rd[0] + 1u;
    f32x16 zt;
#pragma unroll
    for (int i = 0; i < 16; ++i) zt[i] = 0.f;
    u32 xa[4];
    { const u32 row = (helper ? tt * 32 : 0u) + l31;
#pragma unroll
      for (int ks = 0; ks < 4; ++ks) xa[ks] = L_X + row * 128 + (((2 * ks + hf) ^ ((l31 >> 1) & 7)) << 4); }
    u32 wa0 = L_R + hf * 2048 + q4 * 128 + (helper ? (trc ^ ((hw >> 1) << 6)) : (trc ^ ((wid & 1) << 6)) + (wid >> 1) * 1024), wa1 = wa0 + 3 * 16384;
    u32 selw = ((lane & 15) == 4 * (lane >> 4)) ? ONE2 : 0u;
    u32x4 sel = {selw, selw, selw, selw};
    u32 voff = hf * 256 + (lane & 15) * 16;
    PINV(xa[0]); PINV(xa[1]); PINV(xa[2]); PINV(xa[3]); PINV(wa0); PINV(wa1); PINV(sel); PINV(voff);
#define WADDR(s, o) (((s) % 6) < 3 ? wa0 + ((s) % 6) * 16384 + (o) : wa1 + (((s) % 6) - 3) * 16384 + (o))
    SB();
    const u32 prow = tid >> 3;
    const bool vlane = !(lane & 16);
    const u32 fs0 = (NFS * xcd) >> 3, fs1 = (NFS * (xcd + 1)) >> 3, fs = fs0 + rr;
    const bool isF = fs < fs1;
    const rsrc_t rwo = mkrs((const char*)wo + g * 128, 512 * 1024 - g * 128);
    const u32 wb1 = wid * 1024;
    const int sA = (int)tb - 4 + 2 * (int)role, sB = sA + 1;
    const u32 hb = WS_KV + g * 16384 + hw * 4096;
    const u32 bA = sA >= 0 ? hb + (u32)sA * 131072 : hb, bB = sB >= 0 ? hb + (u32)sB * 131072 : hb;
    const u32 cA = sA >= 0 ? bA : WS_END, cB = sB >= 0 ? bB : WS_END;
    u32x4 pm0, pm1, pu0, pu1, k0, k1, k2, k3, v00, v01, v10, v11, v20, v21, v30, v31;
    u32 vwo, aA0, aA1, aB0, aB1, fao; const char* pfp;
    const u32 one_ = (l31 | 4u) == 20u ? ONE2 : 0u;
    auto setup_a = [&]() __attribute__((always_inline)) {
        const int vm = (int)((0xffffffffu >> (31 - l31)) >> (4 * hf));
#pragma unroll
        for (int i = 0; i < 4; ++i) { const int c0 = (2 * i & 3) + 8 * (2 * i >> 2);
            pm0[i] = ((u32)__builtin_amdgcn_sbfe(vm, c0, 1) & 0xffffu) | ((u32)__builtin_amdgcn_sbfe(vm, c0 + 1, 1) & 0xffff0000u);
            pm1[i] = ((u32)__builtin_amdgcn_sbfe(vm, c0 + 16, 1) & 0xffffu) | ((u32)__builtin_amdgcn_sbfe(vm, c0 + 17, 1) & 0xffff0000u); }
        { const u32 al = role ? ~0u : 0u; pu0 = ~pm0 | al; pu1 = ~pm1 | al; }
        PINV(pm0); PINV(pm1); PINV(pu0); PINV(pu1); SB();
    };
    auto setup_b = [&]() __attribute__((always_inline)) {
        vwo = prow * 1024 + wsw;
        if (wid < 2) pfp = (wid ? (const char*)wu : (const char*)wg) + (size_t)(rr * 16 + (lane >> 2)) * (FF * 2) + fs0 * 64 + min(p4 * 128u, (fs1 - fs0) * 64u - 4u);
        else pfp = (const char*)wd + (size_t)fs0 * 32768 + min((rr * 64 + lane) * 128u, (fs1 - fs0) * 32768u - 128u);
        aA0 = (lane * 16) & (sA >= 0 ? 0x3f0u : 0x200u); aA1 = aA0 + (sA >= 0 ? 1024u : 0u);
        aB0 = (lane * 16) & (sB >= 0 ? 0x3f0u : 0x200u); aB1 = aB0 + (sB >= 0 ? 1024u : 0u);
        fao = (u32)max((lane & 1) ? sB : sA, 0) * 4096 + g * 512 + (hw >> 1) * 128 + (lane & 2) * 128;
        PINV(vwo); PINV(pfp); PINV(aA0); PINV(aA1); PINV(aB0); PINV(aB1); PINV(fao); SB();
    };
    auto setup_c = [&]() __attribute__((always_inline)) {
        const u32 oA = sA >= 0 ? one_ : 0u, oB = sB >= 0 ? one_ : 0u;
        v00 = v01 = v10 = v11 = (u32x4){oA, oA, oA, oA}; v20 = v21 = v30 = v31 = (u32x4){oB, oB, oB, oB};
        PINV(v00); PINV(v01); PINV(v10); PINV(v11); PINV(v20); PINV(v21); PINV(v30); PINV(v31); SB();
    };
    u32 pf2 = 0;
#define TABLD(dst, tile) do { _Pragma("nounroll") for (u32 spins = 0; spins < SPIN_CAP; ++spins) { MEMF(); dst = LD128(l31 * 16, WS_TAB + tb * 1024 + (tile) * 512); if (__all(dst.y == tag && dst.w == tag)) break; } } while (0)

    if (helper) {
#define HB(n) asm volatile("s_waitcnt vmcnt(" #n ")\n\ts_barrier" ::: "memory")
        XD(1); XD(2); WD(2);
        HB(8); XD(3); WD(3);
        PINV(xr0); PINV(xr1); PINV(pfd); PINS(tag);
        HB(12); XD(4); WD(4);
        {
            float sx = 0.f, sq = 0.f, tx = 0.f, tq = 0.f;
#pragma unroll
            for (int e = 0; e < 4; ++e) { sx = dot2(xr0[e], ONE2, sx); sq = dot2(xr0[e], xr0[e], sq); tx = dot2(xr1[e], ONE2, tx); tq = dot2(xr1[e], xr1[e], tq); }
            auto r1 = __builtin_amdgcn_permlane32_swap(__float_as_uint(sx), __float_as_uint(tx), false, false);
            auto r2 = __builtin_amdgcn_permlane32_swap(__float_as_uint(sq), __float_as_uint(tq), false, false);
            sx = sum32(__uint_as_float(r1[0]) + __uint_as_float(r1[1])); sq = sum32(__uint_as_float(r2[0]) + __uint_as_float(r2[1]));
            const float nmean = sx * (-1.f / 512.f);
            const float rs = rstd_of(sq * (1.f / 512.f) - nmean * nmean);
            const u32x4 tv = {__float_as_uint(rs), tag, pk(nmean, 0.f), tag};
            if (l31 == 31) ST128(tv, hf * 16, WS_TAB + (wgi * 8 + hw * 2) * 16);
            SB();
        }
        HB(13); XD(5); WD(5); setup_a();
        HB(13); XD(6); WD(6); setup_b();
        HB(12); XD(7); WD(7);
        HB(12); WD(8); setup_c();
        HB(10);
        HB(4); WD(9);
        asm volatile("s_barrier" ::: "memory");
        WD(10); WD(11);
        u32x4 tabv;
        TABLD(tabv, tt);
        waitbar<0>();
        f32x16 acc = zt; f32x4 cq = {0.f, 0.f, 0.f, 0.f};
        u32x4 xf[8], wf[8];
#define QRD(n) do { xf[(n) & 7] = lds128(xa[(n) & 3] + ((n) >> 2) * 8192); \
            wf[(n) & 7] = trfrag(WADDR(8 + (n) / 8, (((n) >> 2) & 1) * 1024 + ((n) & 3) * 4096), WADDR(8 + (n) / 8, (((n) >> 2) & 1) * 1024 + ((n) & 3) * 4096 + 512)); SB(); } while (0)
        QRD(0); QRD(1); QRD(2); QRD(3); QRD(4);
#pragma unroll
        for (int n = 0; n < 32; ++n) {
            cq = MFMA16(sel, wf[n & 7], cq); PINV(cq); SB();
            MF(acc, wf[n & 7], xf[n & 7], acc);
            if (n + 5 < 32) QRD(n + 5);
        }
        const u32 ch = pk(cq[0], cq[0]);
        acc = MFMA32(((u32x4){ch, pk(cq[0] - bfhi(ch), 0.f), 0u, 0u}), ((u32x4){tabv.z, tabv.z & 0xffffu, 0u, 0u}), acc);
        const float qs = __uint_as_float(tabv.x) * (0.25f * 1.4426950408889634f);
#pragma unroll
        for (int i = 0; i < 16; ++i) acc[i] *= qs;
        sts128(L_Q + (4 * (hw >> 1) + tt) * 1024 + lane * 16, pack8(acc, 0));
        sts128(L_Q + (4 * (hw >> 1) + 2 + tt) * 1024 + lane * 16, pack8(acc, 8));
    } else {
        f32x16 a0 = zt, a1 = zt; f32x4 cw = {0.f, 0.f, 0.f, 0.f};
        u32x4 x0[5], x1[5], wf[5], tab0, tab1;
        u32 go, vtw, vtr;
        go = wid < 2 ? (2 * wid) * 4096 + lane * 16 : (2 * (wid - 2) + blk) * 4096 + 2048 + voff;
        vtw = L_Q + (wid & 1) * 4608 + l31 * 72 + hf * 8; vtr = L_Q + (wid & 1) * 4608 + (4 * hf + q4) * 72 + blk * 32 + p4 * 8;
        const u32 gs = wid < 2 ? 4096u : 512u;
        u32 go1 = go + 1024;
        u32 tbo = l31 * 16;
        PINV(go); PINV(go1); PINV(vtw); PINV(vtr); PINV(tbo);
        WD(1);
        PINS(tag);
#define GRW(n) do { wf[(n) % 5] = trfrag(WADDR((n) >> 2, ((n) & 3) * 4096), WADDR((n) >> 2, ((n) & 3) * 4096 + 512)); SB(); } while (0)
#define GRX(n) do { x0[(n) % 5] = lds128(xa[(n) & 3] + ((n) >> 2) * 8192); x1[(n) % 5] = lds128(xa[(n) & 3] + ((n) >> 2) * 8192 + 4096); SB(); } while (0)
        sfor<0, 32 + DG>([&](auto ic) __attribute__((always_inline)) {
            constexpr int n = decltype(ic)::value;
            if constexpr (n < 32 && (n & 3) == 0) {
                if constexpr (n == 0) asm volatile("s_waitcnt vmcnt(4)\n\ts_barrier" ::: "memory");
                else if constexpr (n == 4) asm volatile("s_waitcnt vmcnt(0)\n\ts_barrier" ::: "memory");
                else asm volatile("s_barrier" ::: "memory");
            }
            if constexpr (n == 28 + DG) asm volatile("s_barrier" ::: "memory");
            if constexpr (n == 21) asm volatile("buffer_load_dwordx4 %0, %2, %3, %4 offen sc1\n\tbuffer_load_dwordx4 %1, %2, %3, %4 offen offset:512 sc1"
                                                : "=&v"(tab0), "=&v"(tab1) : "v"(tbo), "s"(wsr), "s"(WS_TAB + tb * 1024) : "memory");
            if constexpr (n >= DG) { cw = MFMA16(sel, wf[(n - DG) % 5], cw); PINV(cw); SB(); }
            if constexpr (n < 32) GRW(n);
            if constexpr (n >= DG) MF(a0, wf[(n - DG) % 5], x0[(n - DG) % 5], a0);
            if constexpr (n < 32) GRX(n);
            if constexpr (n >= DG) MF(a1, wf[(n - DG) % 5], x1[(n - DG) % 5], a1);
        });
        asm volatile("s_waitcnt vmcnt(0)" : "+v"(tab0), "+v"(tab1) :: "memory");
        if (__ballot(((tab0.y ^ tag) | (tab0.w ^ tag) | (tab1.y ^ tag) | (tab1.w ^ tag)) != 0) != 0) { TABLD(tab0, 0); TABLD(tab1, 1); }
        const u32 ch = pk(cw[0], cw[0]);
        const u32x4 cf = {ch, pk(cw[0] - bfhi(ch), 0.f), 0u, 0u};
        MF(a0, cf, ((u32x4){tab0.z, tab0.z & 0xffffu, 0u, 0u}), a0);
        MF(a1, cf, ((u32x4){tab1.z, tab1.z & 0xffffu, 0u, 0u}), a1);
        const float r0_ = __uint_as_float(tab0.x), r1_ = __uint_as_float(tab1.x);
#pragma unroll
        for (int i = 0; i < 16; ++i) { a0[i] *= r0_; }
        u32x4 f0 = pack8(a0, 0), f1 = pack8(a0, 8);
#pragma unroll
        for (int i = 0; i < 16; ++i) { a1[i] *= r1_; }
        u32x4 f2 = pack8(a1, 0), f3 = pack8(a1, 8);
        if (wid >= 2) {
            sts64(vtw, (u32x2){f0.x, f0.y}); sts64(vtw + 16, (u32x2){f0.z, f0.w}); sts64(vtw + 32, (u32x2){f1.x, f1.y}); sts64(vtw + 48, (u32x2){f1.z, f1.w});
            sts64(vtw + 2304, (u32x2){f2.x, f2.y}); sts64(vtw + 2320, (u32x2){f2.z, f2.w}); sts64(vtw + 2336, (u32x2){f3.x, f3.y}); sts64(vtw + 2352, (u32x2){f3.z, f3.w});
            asm volatile("" ::: "memory");
            f0 = trfrag(vtr, vtr + 8 * 72); f1 = trfrag(vtr + 16 * 72, vtr + 24 * 72);
            f2 = trfrag(vtr + 2304, vtr + 2304 + 8 * 72); f3 = trfrag(vtr + 2304 + 16 * 72, vtr + 2304 + 24 * 72);
        }
        ST128(f0, go, WS_KV + wgi * 16384); ST128(f1, go, WS_KV + wgi * 16384 + gs);
        ST128(f2, go1, WS_KV + wgi * 16384); ST128(f3, go1, WS_KV + wgi * 16384 + gs);
        BAR();
        sts128(L_S2 + go, f0); sts128(L_S2 + go + gs, f1); sts128(L_S2 + go + 1024, f2); sts128(L_S2 + go + gs + 1024, f3);
        setup_b();
        WAITV(0);
        if (lane < 8) ST128(((u32x4){tag, tag, tag, tag}), lane * 16, WS_FLAGA + wgi * 512 + wid * 128);
        if (wid == 0) asm volatile("global_load_dword %0, %1, off" : "+v"(pf2) : "v"(pfp) : "memory");
        if (wid & 1) {
            const u32 to = wid == 3 ? WS_STAT + tb * 8192 + lane * 128 : (hf ? WS_FLAGB + tb * 1024 + l7 * 128 : WS_CAND + xcd * 4096 + l31 * 128);
            asm volatile("buffer_load_dword %0, %1, %2, 0 offen sc1" : "+v"(pf2) : "v"(to), "s"(wsr) : "memory");
        }
        setup_a(); setup_c();
    }
    SB();
    const u32x4 xres = lds128(L_X + ((g - rot) & 7) * 8192 + prow * 128 + ((l7 ^ ((prow >> 1) & 7)) << 4));
    BAR();
    const u32x4 qown = lds128(L_Q + hw * 2048 + role * 1024 + lane * 16), qoth = lds128(L_Q + hw * 2048 + (role ^ 1) * 1024 + lane * 16);
    const u32x4 kL = lds128(L_S2 + hw * 4096 + role * 1024 + lane * 16);
    u32x4 vL0 = {one_, one_, one_, one_}, vL1 = vL0;
    if (vlane) { vL0 = lds128(L_S2 + hw * 4096 + 2048 + role * 1024 + voff); vL1 = lds128(L_S2 + hw * 4096 + 2048 + role * 1024 + 512 + voff); }
    u32 exo = L_EX + wid * 2560 + lane * 16;
    PINV(exo);
#define WOD(c) dma1<L_X + (c) * 8192>(wb1, vwo, rwo, XOFF(c) * 512)
#define WV(n, x) asm volatile("s_waitcnt vmcnt(" #n ")" : "+v"(x) :: "memory")
    u32x4 of, wrm, wrv;
    {
        f32x16 sa0, sa1, oacc, ooth;
#define EXPH(S, o) do { _Pragma("unroll") for (int i = 0; i < 8; ++i) S[(o) + i] = __builtin_amdgcn_exp2f(S[(o) + i]); } while (0)
#define TILE(O, S, V0, V1, OIN, NEXT, EX0, EX1) do { NEXT; EXPH(S, 0); u32x4 p_ = pack8(S, 0); EX0; MF(O, V0, p_, OIN); EXPH(S, 8); p_ = pack8(S, 8); EX1; MF(O, V1, p_, O); } while (0)
        u32 kf = tag; PINV(kf);
        asm volatile("s_mov_b64 exec, 15\n\tbuffer_load_dword %0, %1, %2, %3 offen sc1\n\ts_mov_b64 exec, -1" : "+v"(kf) : "v"(fao), "s"(wsr), "s"(WS_FLAGA) : "memory");
        MF(sa0, kL, qown, zt);
        TILE(oacc, sa0, vL0, vL1, zt, MF(sa1, kL, qoth, zt), p_ &= pm0, p_ &= pm1);
        if (role == 0) TILE(ooth, sa1, vL0, vL1, zt, , , ); else asm volatile("" : "=v"(ooth));
        WV(0, kf);
        if (__ballot(kf != tag) != 0) {
            _Pragma("nounroll") for (u32 spins = 0; spins < SPIN_CAP; ++spins) { MEMF();
                u32 f = tag;
                if (lane < 4) f = LD32(fao, WS_FLAGA);
                if (__ballot(f != tag) == 0) break;
            }
        }
        asm volatile("buffer_load_dwordx4 %0, %4, %8, %9 offen sc1\n\tbuffer_load_dwordx4 %1, %5, %8, %9 offen sc1\n\t"
                     "buffer_load_dwordx4 %2, %6, %8, %10 offen sc1\n\tbuffer_load_dwordx4 %3, %7, %8, %10 offen sc1"
                     : "=&v"(k0), "=&v"(k1), "=&v"(k2), "=&v"(k3) : "v"(aA0), "v"(aA1), "v"(aB0), "v"(aB1), "s"(wsr), "s"(bA), "s"(bB) : "memory");
        asm volatile("s_mov_b64 exec, %12\n\t"
                     "buffer_load_dwordx4 %0, %8, %9, %10 offen offset:2048 sc1\n\tbuffer_load_dwordx4 %1, %8, %9, %10 offen offset:2560 sc1\n\t"
                     "buffer_load_dwordx4 %2, %8, %9, %10 offen offset:3072 sc1\n\tbuffer_load_dwordx4 %3, %8, %9, %10 offen offset:3584 sc1\n\t"
                     "buffer_load_dwordx4 %4, %8, %9, %11 offen offset:2048 sc1\n\tbuffer_load_dwordx4 %5, %8, %9, %11 offen offset:2560 sc1\n\t"
                     "buffer_load_dwordx4 %6, %8, %9, %11 offen offset:3072 sc1\n\tbuffer_load_dwordx4 %7, %8, %9, %11 offen offset:3584 sc1\n\t"
                     "s_mov_b64 exec, -1"
                     : "+v"(v00), "+v"(v01), "+v"(v10), "+v"(v11), "+v"(v20), "+v"(v21), "+v"(v30), "+v"(v31)
                     : "v"(voff), "s"(wsr), "s"(cA), "s"(cB), "s"(0x0000ffff0000ffffull) : "memory");
        asm volatile("s_waitcnt vmcnt(10)" : "+v"(k0), "+v"(k1) :: "memory");
        if (role) { MF(sa1, k0, qoth, zt); TILE(ooth, sa1, v00, v01, zt, MF(sa0, k1, qoth, zt), WV(7, p_), WV(6, p_)); }
        else MF(sa0, k1, qoth, zt);
        asm volatile("s_waitcnt vmcnt(8)" : "+v"(k2), "+v"(k3) :: "memory");
        TILE(ooth, sa0, v10, v11, ooth, MF(sa1, k2, qoth, zt), p_ &= pu0; WV(5, v10), p_ &= pu1; WV(4, v11));
        PINV(v00); PINV(v01);
        TILE(ooth, sa1, v20, v21, ooth, MF(sa0, k3, qoth, zt), WV(3, v20), WV(0, v21));
        PINV(v30); PINV(v31);
        TILE(ooth, sa0, v30, v31, ooth, MF(sa1, k0, qown, zt), WOD(0), WOD(1));
        if (wid == 1 || wid == 2) asm volatile("global_load_dword %0, %1, off" : "+v"(pf2) : "v"(pfp) : "memory");
        asm volatile("global_load_dwordx4 %0, %2, %4\n\tglobal_load_dwordx4 %1, %3, %4" : "=&v"(wrm), "=&v"(wrv) : "v"(g * 128 + l7 * 16), "v"(lane * 16), "s"(wr) : "memory");
        TILE(oacc, sa1, v00, v01, oacc, MF(sa0, k1, qown, zt), p_ &= pu0;
             stsf4(exo, (f32x4){ooth[0], ooth[1], ooth[2], ooth[3]}); stsf4(exo + 1024, (f32x4){ooth[4], ooth[5], ooth[6], ooth[7]}); stsf(exo - lane * 12 + 2048, ooth[8]), p_ &= pu1);
        BAR();
        TILE(oacc, sa0, v10, v11, oacc, MF(sa1, k2, qown, zt), , );
        TILE(oacc, sa1, v20, v21, oacc, MF(sa0, k3, qown, zt), , );
        const u32 pe = L_EX + (wid ^ 4) * 2560 + lane * 16;
        const f32x4 o0 = ldsf4(pe), o1 = ldsf4(pe + 1024);
        const float l2 = ldsf(pe - lane * 12 + 2048);
        SB();
        TILE(oacc, sa0, v30, v31, oacc, , , );
        const float lsum = oacc[8] + l2;
        const float inv = __builtin_amdgcn_rcpf(lsum);
        float on[8];
#pragma unroll
        for (int i = 0; i < 4; ++i) { on[i] = oacc[i] + o0[i]; on[4 + i] = oacc[4 + i] + o1[i]; }
        const float osum = ((__builtin_fabsf(on[0]) + __builtin_fabsf(on[1])) + (__builtin_fabsf(on[2]) + __builtin_fabsf(on[3]))) + ((__builtin_fabsf(on[4]) + __builtin_fabsf(on[5])) + (__builtin_fabsf(on[6]) + __builtin_fabsf(on[7])));
        if (__builtin_expect(__all(lsum > 0x1p-90f && lsum < 0x1p90f && osum < 0x1p127f), 1)) {
            of = (u32x4){pk(on[0] * inv, on[1] * inv), pk(on[2] * inv, on[3] * inv), pk(on[4] * inv, on[5] * inv), pk(on[6] * inv, on[7] * inv)};
        } else {
            const u32 fo = (u32)max((int)tb - (int)min(lane >> 2, 4u), 0) * 4096 + g * 512 + p4 * 128;
            _Pragma("nounroll") for (u32 spins = 0; spins < SPIN_CAP; ++spins) { MEMF(); if (__all(LD32(fo, WS_FLAGA) == tag)) break; __builtin_amdgcn_s_sleep(1); }
            __builtin_amdgcn_fence(__ATOMIC_ACQUIRE, "wavefront");
            float m = -__builtin_inff();
            oacc = zt;
            _Pragma("nounroll") for (int it = 0; it < 18; ++it) {
                const int kt = it >= 9 ? it - 9 : it, T = (int)(2 * tb + role) - 8 + kt;
                if (it == 9) m = xhalf_max(m);
                if (T < 0) continue;
                const u32 base = hb + (u32)(T >> 1) * 131072 + (u32)(T & 1) * 1024;
                const u32x4 kf = LD128(lane * 16, base);
                f32x16 sc = MFMA32(kf, qown, zt);
#pragma unroll
                for (int i = 0; i < 16; ++i) { const bool lo = (i & 3) + 8 * (i >> 2) + 4 * hf <= l31; if ((kt == 0 && lo) || (kt == 8 && !lo)) sc[i] = -__builtin_inff(); }
                if (it < 9) m = max16(sc, m);
                else {
                    u32x4 v0 = {ONE2, ONE2, ONE2, ONE2}, v1 = v0;
                    if (vlane) { v0 = LD128(voff + 2048, base); v1 = LD128(voff + 2560, base); }
#pragma unroll
                    for (int i = 0; i < 16; ++i) sc[i] = __builtin_amdgcn_exp2f(sc[i] - m);
                    oacc = MFMA32(v0, pack8(sc, 0), oacc); oacc = MFMA32(v1, pack8(sc, 8), oacc);
                }
            }
            const float iv = __builtin_amdgcn_rcpf(oacc[8]);
            of = (u32x4){pk(oacc[0] * iv, oacc[1] * iv), pk(oacc[2] * iv, oacc[3] * iv), pk(oacc[4] * iv, oacc[5] * iv), pk(oacc[6] * iv, oacc[7] * iv)};
        }
    }
    ST128(of, lane * 16, WS_O + ((tb * 2 + role) * 32 + g * 4 + hw) * 1024);
    waitbar<0>();
    PINV(pf2); PINV(wrm); PINV(wrv);
    if (tid < 8) ST128(((u32x4){tag, tag, tag, tag}), tid * 16, WS_FLAGB + wgi * 128);

    const u32 gq = lane >> 4;
    float wsum = 0.f;
    u32 ra0 = prow * 256 + (((2 * l7) ^ (prow & 7)) << 4) + L_M, ra1 = ra0 ^ 16;
    float xr[8] = {bflo(xres.x), bfhi(xres.x), bflo(xres.y), bfhi(xres.y), bflo(xres.z), bfhi(xres.z), bflo(xres.w), bfhi(xres.w)};
#pragma unroll
    for (int i = 0; i < 8; ++i) PINV(xr[i]);
    PINV(ra0); PINV(ra1);
    if (wid < 4) {
        { float s_ = dot2(wrv[0], ONE2, 0.f); s_ = dot2(wrv[1], ONE2, s_); s_ = dot2(wrv[2], ONE2, s_); s_ = dot2(wrv[3], ONE2, s_);
          wsum = __uint_as_float(__builtin_amdgcn_readlane(__float_as_uint(sum64(s_)), 63)); PINS(wsum); }
        u32 b0 = L_X + ft * 32768 + (4 * hf + q4) * 128 + trc, b1 = b0 ^ 64;
        u32 pb = L_M + ft * 16384 + (tt * 32 + l31) * 256 + ((hf ^ l7) << 4);
        u32 pb1 = pb ^ 32, pb2 = pb ^ 64, pb3 = pb ^ 96;
        PINV(b0); PINV(b1); PINV(pb); PINV(pb1); PINV(pb2); PINV(pb3);
        u32 fo = (tb * 8 + ((ft * 4 + (lane & 3) + rot) & 7)) * 128, l16 = lane * 16;
        u32 so[4];
#pragma unroll
        for (int c = 0; c < 4; ++c) { so[c] = WS_O + (tb * 2 + tt) * 32768 + ((ft * 4 + c + rot) & 7) * 4096; PINS(so[c]); }
        PINV(fo); PINV(l16);
        u32 bf = tag; PINV(bf);
        __builtin_amdgcn_s_sleep(BSLEEP);
        asm volatile("s_mov_b64 exec, 15\n\tbuffer_load_dword %0, %1, %2, %3 offen sc1\n\ts_mov_b64 exec, -1" : "+v"(bf) : "v"(fo), "s"(wsr), "s"(WS_FLAGB) : "memory");
        asm volatile("s_barrier" ::: "memory");
        u32x4 w0[5], w1[5];
#define WORD(i) do { w0[(i) % 5] = trfrag(b0 + (i) * 2048, b0 + (i) * 2048 + 1024); w1[(i) % 5] = trfrag(b1 + (i) * 2048, b1 + (i) * 2048 + 1024); SB(); } while (0)
        WORD(0); WORD(1); WORD(2);
        WV(0, bf);
        if (__ballot(bf != tag) != 0) {
            _Pragma("nounroll") for (u32 spins = 0; spins < SPIN_CAP; ++spins) { MEMF();
                u32 f = tag;
                if (lane < 4) f = LD32(fo, WS_FLAGB);
                if (__ballot(f != tag) == 0) break;
            }
        }
        u32x4 og[16];
#define OLD(i) asm volatile("buffer_load_dwordx4 %0, %1, %2, %3 offen offset:%4 sc1" : "=v"(og[i]) : "v"(l16), "s"(wsr), "s"(so[(i) >> 2]), "i"(((i) & 3) * 1024) : "memory")
        OLD(0); OLD(1); OLD(2); OLD(3); OLD(4); OLD(5); OLD(6); OLD(7);
        f32x16 a0 = zt, a1 = zt;
        sfor<0, 16>([&](auto ic) __attribute__((always_inline)) {
            constexpr int i = decltype(ic)::value;
            asm volatile("s_waitcnt vmcnt(%1)" : "+v"(og[i]) : "i"(i < 8 ? 7 : 15 - i) : "memory");
            if constexpr (i + 8 < 16) OLD(i + 8);
            if constexpr (i == 7) asm volatile("s_barrier" ::: "memory");
            MF(a0, w0[i % 5], og[i], a0);
            if constexpr (i + 3 < 16) WORD(i + 3);
            MF(a1, w1[i % 5], og[i], a1);
        });
        stsf4(pb, (f32x4){a0[0], a0[1], a0[2], a0[3]}); stsf4(pb1, (f32x4){a0[4], a0[5], a0[6], a0[7]});
        stsf4(pb2, (f32x4){a0[8], a0[9], a0[10], a0[11]}); stsf4(pb3, (f32x4){a0[12], a0[13], a0[14], a0[15]});
        stsf4(pb + 128, (f32x4){a1[0], a1[1], a1[2], a1[3]}); stsf4(pb1 + 128, (f32x4){a1[4], a1[5], a1[6], a1[7]});
        stsf4(pb2 + 128, (f32x4){a1[8], a1[9], a1[10], a1[11]}); stsf4(pb3 + 128, (f32x4){a1[12], a1[13], a1[14], a1[15]});
    } else {
        {
            const u32 vo0 = r0 * 1024 + wsw, vo1 = vo0 + 8192;
#define WOH(c) do { dma1<L_X + (c) * 8192>(wbx, vo0, rwo, XOFF(c) * 512); dma1<L_X + (c) * 8192 + 1024>(wbx, vo1, rwo, XOFF(c) * 512); } while (0)
            WOH(4); WOH(2); WOH(5); WOH(3); WOH(6); WOH(7);
        }
        WAITV(0);
        const char* fsrc = ((wsw & 64) ? (const char*)wu : (const char*)wg) + (size_t)(hw * 128 + l3) * (FF * 2) + fs * 64 + (wsw & 48);
        const u32 fdst = L_F + hw * 16384;
#define FFD(c) asm volatile("s_add_u32 m0, %0, %2\n\ts_nop 0\n\tglobal_load_lds_dwordx4 %1, off" :: "s"(fdst), "v"(fsrc + (size_t)(c) * 8 * (FF * 2)), "i"((c) * 1024) : "scc", "memory")
        asm volatile("s_barrier\n\ts_barrier" ::: "memory");
        if (isF) { FFD(0); FFD(1); FFD(2); FFD(3); FFD(4); FFD(5); FFD(6); FFD(7); }
    }
    BAR();
    u32x4 hp;
    {
        const f32x4 r0 = ldsf4(ra0) + ldsf4(ra0 + 16384), r1 = ldsf4(ra1) + ldsf4(ra1 + 16384);
        hp = (u32x4){pk(r0[0] + xr[0], r0[1] + xr[1]), pk(r0[2] + xr[2], r0[3] + xr[3]), pk(r1[0] + xr[4], r1[1] + xr[5]), pk(r1[2] + xr[6], r1[3] + xr[7])};
        float s1 = 0.f, s2 = 0.f, s3 = 0.f;
#pragma unroll
        for (int e = 0; e < 4; ++e) { s1 = dot2(hp[e], ONE2, s1); s2 = dot2(hp[e], hp[e], s2); s3 = dot2(hp[e], wrm[e], s3); }
        s1 = sum8(s1); s2 = sum8(s2); s3 = sum8(s3);
        if (l7 == 0) ST128(((u32x4){__float_as_uint(s1), __float_as_uint(s2), __float_as_uint(s3), (tag ^ CHK) ^ __float_as_uint(s1) ^ __float_as_uint(s2) ^ __float_as_uint(s3)}), prow * 16, WS_STAT + wgi * 1024);
    }
    if (isF && helper) {
        const char* fsrc = ((wsw & 64) ? (const char*)wu : (const char*)wg) + (size_t)(hw * 128 + l3) * (FF * 2) + fs * 64 + (wsw & 48);
        const u32 fdst = L_F + hw * 16384;
        FFD(8); FFD(9); FFD(10); FFD(11); FFD(12); FFD(13); FFD(14); FFD(15);
        const rsrc_t rwd = mkrs((const char*)wd + (size_t)fs * 32768, 32768);
        const u32 vo = l3 * 1024 + wsw;
#pragma unroll
        for (int c = 0; c < 8; ++c) dma1<0>(L_X + 32768 + hw * 8192 + c * 1024, vo, rwd, (c & 3) * 8192 + (hw * 2 + (c >> 2)) * 128);
    }
    constexpr u32 L_ST = L_X, L_WIN = L_X + 512, L_STASH = L_X + 1024, L_RED = L_X + 4096;
    if (wid == 0) {
        WAITV(0);
        float s1, s2, s3;
        _Pragma("nounroll") for (u32 spins = 0; spins < SPIN_CAP; ++spins) { MEMF();
            u32x4 a[8];
#pragma unroll
            for (int k = 0; k < 8; ++k) a[k] = LD128(lane * 16 + (k & 3) * 1024, WS_STAT + tb * 8192 + (k >> 2) * 4096);
            u32 bad = 0; s1 = s2 = s3 = 0.f;
#pragma unroll
            for (int k = 0; k < 8; ++k) { bad |= (a[k].x ^ a[k].y ^ a[k].z) ^ (a[k].w ^ (tag ^ CHK)); s1 += __uint_as_float(a[k].x); s2 += __uint_as_float(a[k].y); s3 += __uint_as_float(a[k].z); PINV(bad); PINV(s1); PINV(s2); PINV(s3); }
            if (__ballot(bad != 0) == 0) break;
        }
        const float mu_ = s1 * (1.f / 512.f), mu = rbf(mu_);
        const float rs = rstd_of(s2 * (1.f / 512.f) - mu_ * mu_);
        const u32 key = (fkey((s3 - mu * wsum) * rs) & ~63u) | (63u - lane);
        sts64(L_ST + lane * 8, (u32x2){__float_as_uint(rs), __float_as_uint(-mu * rs)});
        const u32 k1 = wave_umax(key), k2 = wave_umax(key == k1 ? 0u : key);
        if (g == 7) ST128(((u32x4){k1, tag, k2, tag}), l3 * 4096 + l7 * 16, WS_CAND + tb * 128);
        if (lane == 0) sts64(L_WIN, (u32x2){63u - (k1 & 63u), 63u - (k2 & 63u)});
        BAR();
    } else waitbar<0>();
    u32x4 cnd = {0u, tag, 0u, tag};
    if (isF && wid == 7) asm volatile("s_mov_b64 exec, %4\n\tbuffer_load_dwordx4 %0, %1, %2, %3 offen sc1\n\ts_mov_b64 exec, -1" : "+v"(cnd) : "v"(lane * 128), "s"(wsr), "s"(WS_CAND + xcd * 4096), "s"(0xffffffffull) : "memory");
    u32 c1, c2;
    u32x4 ov;
    {
        const u32x2 st = lds64(L_ST + prow * 8), cn = lds64(L_WIN);
        c1 = cn.x; c2 = cn.y;
        const float rs = __uint_as_float(st.x), nb = __uint_as_float(st.y);
        u32x4 hn;
#pragma unroll
        for (int e = 0; e < 4; ++e) hn[e] = pk(bflo(hp[e]) * rs + nb, bfhi(hp[e]) * rs + nb);
        if (prow == c1 || prow == c2) {
            const u32 ci = prow == c1 ? 0u : 1u;
            const u32 ro = ci * 2048 + l7 * 32;
            ST128(((u32x4){hn[0], tag, hn[1], tag}), ro, WS_ROWS + tb * 4096 + g * 256);
            ST128(((u32x4){hn[2], tag, hn[3], tag}), ro + 16, WS_ROWS + tb * 4096 + g * 256);
            sts128(L_STASH + ci * 256 + l7 * 16, hp); sts128(L_STASH + ci * 256 + 128 + l7 * 16, hn);
        }
#pragma unroll
        for (int e = 0; e < 4; ++e) ov[e] = pk(bflo(hp[e]) + bflo(hn[e]), bfhi(hp[e]) + bfhi(hn[e]));
    }
    u16* const orow = out + (size_t)(tb * 64 + prow) * 512 + g * 64 + l7 * 8;
    u32x4 gf[2][4], wdf[4];
    if (isF) {
        const u32 fa_ = L_F + wid * 8192 + (16 * gq + q4) * 128 + ((p4 * 8) ^ ((q4 >> 1) << 6));
#pragma unroll
        for (int s_ = 0; s_ < 2; ++s_)
#pragma unroll
            for (int t = 0; t < 4; ++t) { const u32 a_ = (fa_ ^ ((t & 2) << 5)) + (t & 1) * 32 + s_ * 1024; gf[s_][t] = trfrag(a_, a_ + 512); }
        const u32 db = L_X + 32768 + wid * 4096 + (8 * gq + q4) * 128 + ((p4 * 8) ^ ((q4 >> 1) << 6));
#pragma unroll
        for (int t = 0; t < 4; ++t) { const u32 a_ = (db ^ ((t & 2) << 5)) + (t & 1) * 32; wdf[t] = trfrag(a_, a_ + 512); }
    }
    u32 rw_ = L_RED + wid * 512 + lane * 8, rr_ = L_RED + lane * 8, aw_ = L_RED + 4096 + wid * 128 + lane * 2, ar_ = L_RED + 4096 + wid * 128 + ((lane >> 2) & 1) * 64 + gq * 16,
        ys_ = ((lane >> 2) & 1) * 2048 + gq * 64 + p4 * 16;
    PINV(rw_); PINV(rr_); PINV(aw_); PINV(ar_); PINV(ys_);
    if (wid == 7) {
        asm volatile("s_waitcnt vmcnt(0)" : "+v"(cnd) :: "memory");
        if (!isF || __ballot(((cnd.y ^ tag) | (cnd.w ^ tag)) != 0) != 0) {
            const bool pref = rr == 31;
            if (!isF && !pref) __builtin_amdgcn_s_sleep(20);
            _Pragma("nounroll") for (u32 spins = 0; spins < SPIN_CAP; ++spins) { MEMF();
                if (lane < 32) cnd = LD128(lane * 128, WS_CAND + xcd * 4096);
                const bool bad = ((cnd.y ^ tag) | (cnd.w ^ tag)) != 0;
                if (__ballot(bad) == 0) break;
                if (pref) {
                    const u32 kx = bad ? 0u : cnd.x;
                    const u32 l1 = (u32)__builtin_ctzll(__ballot(kx == wave_umax(kx)));
                    const u32 ky = lane == l1 ? (bad ? 0u : cnd.z) : kx;
                    const u32 l2 = (u32)__builtin_ctzll(__ballot(ky == wave_umax(ky)));
                    const u32 sl = blk ? l2 * 2 + (l2 == l1 ? 1u : 0u) : l1 * 2;
                    if (lane < 32) { const u32 t_ = LD32(sl * 2048 + (lane & 15) * 128, WS_ROWS); asm volatile("" :: "v"(t_)); }
                } else if (!isF) __builtin_amdgcn_s_sleep(8);
            }
        }
        if (b == 255 && lane == 0) __builtin_amdgcn_raw_buffer_store_b32(tag, wsr, 0, WS_EPOCH, SC1);
        const u32 k1 = wave_umax(cnd.x);
        const u32 l1 = (u32)__builtin_ctzll(__ballot(cnd.x == k1));
        const u32 key2 = lane == l1 ? cnd.z : cnd.x;
        const u32 k2 = wave_umax(key2);
        const u32 l2 = (u32)__builtin_ctzll(__ballot(key2 == k2));
        if (lane == 0) sts64(L_WIN + 8, (u32x2){(l1 * 2) | (k1 & ~63u), (l2 * 2 + (l2 == l1 ? 1u : 0u)) | (k2 & ~63u)});
    }
    BAR();
    const u32x2 wn = lds64(L_WIN + 8);
    const u32 w1 = rfl(wn.x), w2 = rfl(wn.y);
    if (isF) {
        const u32 slot = ((lane & 1) ? w2 : w1) & 63u;
        const u32 ro = slot * 2048 + gq * 64;
        u32x4 af0 = {0, 0, 0, 0}, af1 = af0;
        _Pragma("nounroll") for (u32 spins = 0; spins < SPIN_CAP; ++spins) { MEMF();
            u32 bad = 0;
            if ((lane & 14) == 0) {
                const u32x4 a0 = LD128(ro, WS_ROWS + wid * 256), a1 = LD128(ro + 16, WS_ROWS + wid * 256), a2 = LD128(ro + 32, WS_ROWS + wid * 256), a3 = LD128(ro + 48, WS_ROWS + wid * 256);
                af0 = (u32x4){a0.x, a0.z, a1.x, a1.z}; af1 = (u32x4){a2.x, a2.z, a3.x, a3.z};
                bad = (a0.y ^ tag) | (a0.w ^ tag) | (a1.y ^ tag) | (a1.w ^ tag) | (a2.y ^ tag) | (a2.w ^ tag) | (a3.y ^ tag) | (a3.w ^ tag);
            }
            if (__ballot(bad != 0) == 0) break;
        }
        f32x4 d[4];
#pragma unroll
        for (int t = 0; t < 4; ++t) { d[t] = MFMA16(af0, gf[0][t], ((f32x4){0.f, 0.f, 0.f, 0.f})); d[t] = MFMA16(af1, gf[1][t], d[t]); }
        if (lane < 16) {
#pragma unroll
            for (int t = 0; t < 4; ++t) sts64(rw_ + t * 128, (u32x2){__float_as_uint(d[t][0]), __float_as_uint(d[t][1])});
        }
        BAR();
        float gv, uv;
        {
            f32x2 p[8];
#pragma unroll
            for (int k = 0; k < 8; ++k) p[k] = __builtin_bit_cast(f32x2, lds64(rr_ + k * 512));
            const f32x2 q = ((p[0] + p[1]) + (p[2] + p[3])) + ((p[4] + p[5]) + (p[6] + p[7]));
            auto r = __builtin_amdgcn_permlane32_swap(__float_as_uint(q[0]), __float_as_uint(q[1]), false, false);
            gv = rbf(__uint_as_float(r[0])); uv = rbf(__uint_as_float(r[1]));
        }
        const float sg = rbf(gv * __builtin_amdgcn_rcpf(1.f + __builtin_amdgcn_exp2f(-1.4426950408889634f * gv)));
        *(LAS u16*)(uintptr_t)aw_ = (u16)pk(sg * uv, 0.f);
        asm volatile("" ::: "memory");
        const u32x4 a2 = lds128(ar_);
#pragma unroll
        for (int t = 0; t < 4; ++t) d[t] = MFMA16(wdf[t], a2, ((f32x4){0.f, 0.f, 0.f, 0.f}));
        {
            const f32x4 dm = p4 == 0 ? d[0] : (p4 == 1 ? d[1] : (p4 == 2 ? d[2] : d[3]));
            if ((lane & 8) == 0) ST128(((u32x4){pk(dm[0], dm[1]), tag, pk(dm[2], dm[3]), tag}), ys_, WS_Y + fs * 4096 + wid * 256);
        }
    }
    const bool own1 = ((w1 & 63u) >> 1) == tb, own2 = ((w2 & 63u) >> 1) == tb;
    const u32 t1 = own1 ? ((w1 & 1u) ? c2 : c1) : 64u, t2 = own2 ? ((w2 & 1u) ? c2 : c1) : 64u;
    if (isF && !(own1 || own2)) __builtin_amdgcn_s_sleep(10);
    if (prow != t1 && prow != t2) OUTST();
    if (own1 || own2) {
        const u32 wk = tt ? w2 : w1;
        const float rv = rbf(fkey_inv(wk & ~63u));
        float sg = rbf(__builtin_amdgcn_rcpf(1.f + __builtin_amdgcn_exp2f(-1.4426950408889634f * rv)));
        float hh = bflo(*(const LAS u16*)(uintptr_t)(L_STASH + (wk & 1u) * 256 + lane * 2));
        float hq = bflo(*(const LAS u16*)(uintptr_t)(L_STASH + (wk & 1u) * 256 + 128 + lane * 2));
        u16* op = out + (size_t)(tb * 64 + min(tt ? t2 : t1, 63u)) * 512 + g * 64 + lane;
        PINV(sg); PINV(hh); PINV(hq); PINV(op);
        u32 fw_ = L_RED + 8192 + tid * 4, fr_ = L_RED + 8192 + tt * 2048 + ((lane & 1) * 32 + ((lane >> 2) & 3) * 8 + gq * 2 + ((lane >> 1) & 1)) * 4;
        PINV(fw_); PINV(fr_);
        u32 yo[3];
#pragma unroll
        for (int k = 0; k < 3; ++k) { yo[k] = min(wid * 2 + hf + 16u * k, (u32)NFS - 1u) * 4096 + l31 * 8; PINV(yo[k]); }
        const u32 lastm = wid * 2 + hf + 32u < (u32)NFS ? ~0u : 0u;
        if (isF) WAITV(0); else __builtin_amdgcn_s_sleep(DSLEEP);
        u32x2 y[2][3];
        _Pragma("nounroll") for (u32 spins = 0; spins < SPIN_CAP; ++spins) { MEMF();
            u32 bad = 0;
#pragma unroll
            for (int k = 0; k < 3; ++k) { y[0][k] = LD64(yo[k], WS_Y + g * 256 + (own1 ? 0u : 2048u)); y[1][k] = LD64(yo[k], WS_Y + g * 256 + (own2 ? 2048u : 0u)); }
#pragma unroll
            for (int k = 0; k < 3; ++k) bad |= (y[0][k].y ^ tag) | (y[1][k].y ^ tag);
            if (__ballot(bad != 0) == 0) break;
        }
#pragma unroll
        for (int w = 0; w < 2; ++w) {
            const u32 l_ = y[w][2].x & lastm;
            const float e0 = bflo(y[w][0].x) + bflo(y[w][1].x) + bflo(l_), e1 = bfhi(y[w][0].x) + bfhi(y[w][1].x) + bfhi(l_);
            auto r = __builtin_amdgcn_permlane32_swap(__float_as_uint(e0), __float_as_uint(e1), false, false);
            stsf(fw_ + w * 2048, __uint_as_float(r[0]) + __uint_as_float(r[1]));
        }
        BAR();
        if (wid < 2 && (tt ? own2 : own1)) {
            float p[8];
#pragma unroll
            for (int k = 0; k < 8; ++k) p[k] = ldsf(fr_ + k * 256);
            const float y = ((p[0] + p[1]) + (p[2] + p[3])) + ((p[4] + p[5]) + (p[6] + p[7]));
            const float res = hh + rbf(hq + rbf(rbf(y) * sg));
            const float nb_ = __int_as_float(__builtin_amdgcn_update_dpp(0, __float_as_int(res), 0xB1, 0xf, 0xf, true));
            if (!(lane & 1)) asm volatile("global_store_dword %0, %1, off sc1" :: "v"(op), "v"(pk(res, nb_)) : "memory");
        }
    }
}

extern "C" void kernel_launch(void* const* d_in, const int* in_sizes, int n_in, void* d_out, int out_size, void* d_ws, size_t ws_size, hipStream_t stream) {
    (void)in_sizes; (void)n_in; (void)out_size; (void)ws_size;
    (void)hipFuncSetAttribute((const void*)fused_block, hipFuncAttributeMaxDynamicSharedMemorySize, LDS_BYTES);
    hipLaunchKernelGGL(fused_block, dim3(256), dim3(512), LDS_BYTES, stream,
                       (const u16*)d_in[0], (const u16*)d_in[1], (const u16*)d_in[2], (const u16*)d_in[4], (const u16*)d_in[5],
                       (const u16*)d_in[6], (const u16*)d_in[7], (u16*)d_out, (const u32*)d_ws, (char*)d_ws);
}
```
